```python
import torch

def _softmax(x, axis=-1):
    return torch.softmax(x, dim=axis)
def _gelu(x):
    return torch.nn.functional.gelu(x, approximate="tanh")
def _silu(x):
    return torch.nn.functional.silu(x)
def _sigmoid(x):
    return torch.sigmoid(x)
def _layernorm(x, axis=-1, eps=1e-5):
    m = x.mean(dim=axis, keepdim=True)
    v = x.var(dim=axis, keepdim=True, unbiased=False)
    return (x - m) / torch.sqrt(v + eps)
def _scatter_add(tgt, idx, src, axis=0):
    return tgt.clone().index_add_(axis, idx, src)
def _segment_sum(src, seg, n):
    out = torch.zeros((n,) + src.shape[1:], dtype=src.dtype)
    return out.index_add_(0, seg, src)
def _pad(x, pw):
    flat = tuple(v for pair in reversed(pw) for v in pair)
    return torch.nn.functional.pad(x, flat)

kernel_name = 'gather_scatter__edge_featured_mpnn_gatedgcn__graph'

def setup_inputs():

    _w = (lambda a: (lambda t: t.to(torch.bfloat16) if t.is_floating_point() else t)(torch.as_tensor(a)))
    N, deg, D, De = 50000, 16, 64, max(16, 1)
    E = N * deg
    return {
        "x": _w((lambda *s: torch.randn(*s, dtype=torch.bfloat16) * 0.4)(N, D)),
        "e_attr": _w((lambda *s: torch.randn(*s, dtype=torch.bfloat16) * 0.8)(E, De)),
        "src": _w((lambda lo, hi, *s: torch.randint(lo, hi, s))(0, N, E).to(torch.int64)),
        "dst": _w((lambda lo, hi, *s: torch.randint(lo, hi, s))(0, N, E).to(torch.int64)),
        "w_a": _w((lambda *s: torch.randn(*s, dtype=torch.bfloat16) * 0.8)(D, D)),
        "w_b": _w((lambda *s: torch.randn(*s, dtype=torch.bfloat16) * 0.4)(D, D)),
        "w_c": _w((lambda *s: torch.randn(*s, dtype=torch.bfloat16) * 0.8)(De, D)),
        "w_u": _w((lambda *s: torch.randn(*s, dtype=torch.bfloat16) * 0.05)(D, D)),
        "w_v": _w((lambda *s: torch.randn(*s, dtype=torch.bfloat16) * 0.05)(D, D)),
    }

def reference(x, e_attr, src, dst, w_a, w_b, w_c, w_u, w_v):
    N, D = x.shape
    hv = x @ w_v
    g = x[dst] @ w_a + x[src] @ w_b + e_attr @ w_c
    eta = _sigmoid(g)
    denom = _segment_sum(eta, dst, N) + 1e-6
    eta_n = eta / denom[dst]
    agg = _segment_sum(eta_n * hv[src], dst, N)
    z = x @ w_u + agg
    relu_z = torch.where(z > 0, z, 0 * z)
    return x + relu_z
```

```cpp
#include <hip/hip_runtime.h>
#include <hip/hip_bf16.h>
#include <stdint.h>

static __device__ __forceinline__ float bf2f(unsigned short u) { return __uint_as_float(((unsigned)u) << 16); }

__global__ void proj_kernel(const unsigned short* x, const unsigned short* wa, const unsigned short* wb,
                            const unsigned short* wv, const unsigned short* wu, float* proj, float* acc, int N) {
  int n = blockIdx.x;
  int t = threadIdx.x;
  int m = t >> 6, f = t & 63;
  const unsigned short* w = m == 0 ? wa : m == 1 ? wb : m == 2 ? wv : wu;
  float s = 0.f;
  for (int k = 0; k < 64; ++k) s += bf2f(x[n * 64 + k]) * bf2f(w[k * 64 + f]);
  proj[(size_t)n * 256 + t] = s;
  if (t < 128) acc[(size_t)n * 128 + t] = 0.f;
}

__global__ void edge_kernel(const unsigned short* ea, const int* src, const int* dst, const unsigned short* wc,
                            const float* proj, float* acc, int E) {
  int e = blockIdx.x * 4 + (threadIdx.x >> 6);
  int f = threadIdx.x & 63;
  if (e >= E) return;
  int s = src[e], d = dst[e];
  float g = proj[(size_t)d * 256 + f] + proj[(size_t)s * 256 + 64 + f];
  for (int k = 0; k < 16; ++k) g += bf2f(ea[(size_t)e * 16 + k]) * bf2f(wc[k * 64 + f]);
  float eta = 1.f / (1.f + __expf(-g));
  atomicAdd(&acc[(size_t)d * 128 + f], eta);
  atomicAdd(&acc[(size_t)d * 128 + 64 + f], eta * proj[(size_t)s * 256 + 128 + f]);
}

__global__ void fin_kernel(const unsigned short* x, const float* proj, const float* acc, __hip_bfloat16* out, int N) {
  int i = blockIdx.x * 256 + threadIdx.x;
  if (i >= N * 64) return;
  int n = i >> 6, f = i & 63;
  float agg = acc[(size_t)n * 128 + 64 + f] / (acc[(size_t)n * 128 + f] + 1e-6f);
  float z = proj[(size_t)n * 256 + 192 + f] + agg;
  out[i] = __float2bfloat16(bf2f(x[i]) + (z > 0.f ? z : 0.f));
}

extern "C" void kernel_launch(void* const* d_in, const int* in_sizes, int n_in,
                              void* d_out, int out_size, void* d_ws, size_t ws_size,
                              hipStream_t stream) {
  const unsigned short* x = (const unsigned short*)d_in[0];
  const unsigned short* ea = (const unsigned short*)d_in[1];
  const int* src = (const int*)d_in[2];
  const int* dst = (const int*)d_in[3];
  const unsigned short* wa = (const unsigned short*)d_in[4];
  const unsigned short* wb = (const unsigned short*)d_in[5];
  const unsigned short* wc = (const unsigned short*)d_in[6];
  const unsigned short* wu = (const unsigned short*)d_in[7];
  const unsigned short* wv = (const unsigned short*)d_in[8];
  int N = in_sizes[0] / 64, E = in_sizes[2];
  float* proj = (float*)d_ws;
  float* acc = proj + (size_t)N * 256;
  proj_kernel<<<N, 256, 0, stream>>>(x, wa, wb, wv, wu, proj, acc, N);
  edge_kernel<<<(E + 3) / 4, 256, 0, stream>>>(ea, src, dst, wc, proj, acc, E);
  fin_kernel<<<(N * 64 + 255) / 256, 256, 0, stream>>>(x, proj, acc, (__hip_bfloat16*)d_out, N);
}
```

```cpp
#include <hip/hip_runtime.h>
#include <hip/hip_bf16.h>
#include <stdint.h>
#include <string.h>

typedef unsigned int u32;
typedef unsigned short u16;
using bf16x8 = __attribute__((ext_vector_type(8))) __bf16;
using bf16x4 = __attribute__((ext_vector_type(4))) __bf16;
using i16x4 = __attribute__((ext_vector_type(4))) short;
using i16x8 = __attribute__((ext_vector_type(8))) short;
using f32x16 = __attribute__((ext_vector_type(16))) float;
using f32x8 = __attribute__((ext_vector_type(8))) float;
using f32x2 = __attribute__((ext_vector_type(2))) float;
using bf16x2 = __attribute__((ext_vector_type(2))) __bf16;
using u32x4 = __attribute__((ext_vector_type(4))) u32;
using u32x2 = __attribute__((ext_vector_type(2))) u32;

#define DI __device__ __forceinline__
#define MFMA32(a, b, c) __builtin_amdgcn_mfma_f32_32x32x16_bf16((a), (b), (c), 0, 0, 0)

constexpr int NBK = 256;
constexpr int G1 = 256;
constexpr int CHMAX = 4096;
constexpr int CAPP = 4352;

constexpr int WIMG_WB = 0, WIMG_WV = 512, WIMG_WC = 1024, WIMG_WA = 1152, WIMG_WU = 1664, WIMG_UNITS = 2176;

constexpr int L_STG = 0;
constexpr int STG_BUF = 5120, STG_WAVE = 2 * STG_BUF;
constexpr int L_WAU = 81920;
constexpr int L_T1 = L_WAU + 16384;
constexpr int L_T2 = L_T1 + 2176;
constexpr int L_SRC = L_T2 + 2176;
constexpr int L_EID = L_SRC + 4 * CAPP;
constexpr int L_TOF = L_EID + 4 * CAPP;
constexpr int L_HIST = L_TOF + 2 * CAPP;
constexpr int L_PFX = L_HIST + 1024;
constexpr int L_WIN = L_PFX + 1056;
constexpr int L_TOTAL = L_WIN + 256;

static DI float bf2f(u16 u) { return __uint_as_float(((u32)u) << 16); }

__global__ __launch_bounds__(1024) void part_kernel(const int* __restrict__ src, const int* __restrict__ dst,
                                                    u32x2* __restrict__ recbuf, u32* __restrict__ segT,
                                                    const u16* __restrict__ wa, const u16* __restrict__ wb,
                                                    const u16* __restrict__ wc, const u16* __restrict__ wu,
                                                    const u16* __restrict__ wv, u32x4* __restrict__ wimg,
                                                    int E, int CH, int NPB, u32 magic) {
  __shared__ __attribute__((aligned(16))) u32x2 recs[CHMAX];
  __shared__ __attribute__((aligned(16))) u32 hist[NBK];
  __shared__ __attribute__((aligned(16))) u32 offs[NBK];
  const int tid = threadIdx.x;
  const int c = blockIdx.x;
  if (c >= G1) {
    int u = (c - G1) * 1024 + tid;
    if (u < WIMG_UNITS) {
      const u16* w; int v;
      if (u < WIMG_WV) { w = wb; v = u - WIMG_WB; }
      else if (u < WIMG_WC) { w = wv; v = u - WIMG_WV; }
      else if (u < WIMG_WA) { w = wc; v = u - WIMG_WC; }
      else if (u < WIMG_WU) { w = wa; v = u - WIMG_WA; }
      else { w = wu; v = u - WIMG_WU; }
      const int lane = v & 63, ts = v >> 6;
      int t, s;
      if (u >= WIMG_WC && u < WIMG_WA) { t = ts; s = 0; } else { t = ts >> 2; s = ts & 3; }
      const int col = lane & 31, h = lane >> 5;
      u32 r[4];
#pragma unroll
      for (int j = 0; j < 4; ++j) {
        const int k = 16 * s + 8 * h + 2 * j;
        u32 lo = w[k * 64 + 2 * col + t], hi = w[(k + 1) * 64 + 2 * col + t];
        r[j] = lo | (hi << 16);
      }
      u32x4 o = {r[0], r[1], r[2], r[3]};
      wimg[u] = o;
    }
    return;
  }
  const int ebase = c * CH;
  int cnt = E - ebase; cnt = cnt < 0 ? 0 : (cnt > CH ? CH : cnt);
  const int i0 = tid * 4;
  int d4[4], s4[4];
  if (i0 + 4 <= cnt) {
    u32x4 dv = *(const u32x4*)(dst + ebase + i0);
    u32x4 sv = *(const u32x4*)(src + ebase + i0);
#pragma unroll
    for (int j = 0; j < 4; ++j) { d4[j] = (int)dv[j]; s4[j] = (int)sv[j]; }
  } else {
#pragma unroll
    for (int j = 0; j < 4; ++j) {
      const bool ok = i0 + j < cnt;
      d4[j] = ok ? dst[ebase + i0 + j] : -1;
      s4[j] = ok ? src[ebase + i0 + j] : 0;
    }
  }
  if (tid < NBK) hist[tid] = 0;
  __syncthreads();
  u32 bk[4], rk[4];
#pragma unroll
  for (int j = 0; j < 4; ++j) {
    if (d4[j] >= 0) {
      u32 b = __umulhi((u32)d4[j], magic);
      bk[j] = b;
      rk[j] = atomicAdd(&hist[b], 1u);
    }
  }
  __syncthreads();
  if (tid < 64) {
    u32x4 h4 = *(const u32x4*)&hist[4 * tid];
    u32 tot = h4[0] + h4[1] + h4[2] + h4[3];
    u32 inc = tot;
#pragma unroll
    for (int o = 1; o < 64; o <<= 1) { u32 y = __shfl_up(inc, o, 64); if (tid >= o) inc += y; }
    u32 p = inc - tot;
    u32x4 o4 = {p, p + h4[0], p + h4[0] + h4[1], p + h4[0] + h4[1] + h4[2]};
    *(u32x4*)&offs[4 * tid] = o4;
#pragma unroll
    for (int j = 0; j < 4; ++j) segT[(4 * tid + j) * G1 + c] = (o4[j] << 16) | h4[j];
  }
  __syncthreads();
#pragma unroll
  for (int j = 0; j < 4; ++j) {
    if (d4[j] >= 0) {
      u32 dl = (u32)d4[j] - bk[j] * (u32)NPB;
      u32x2 r = {(u32)s4[j], ((u32)(ebase + i0 + j) << 8) | dl};
      recs[offs[bk[j]] + rk[j]] = r;
    }
  }
  __syncthreads();
  u32x4* o = (u32x4*)(recbuf + (size_t)c * CH);
  const u32x4* li = (const u32x4*)recs;
  const int n2 = (cnt + 1) >> 1;
  for (int i = tid; i < n2; i += 1024) o[i] = li[i];
}

static DI void dma16(u32 lds_addr, u32 voff, const void* base) {
  u32 keep;
  asm volatile("s_mov_b32 %0, m0\n\ts_mov_b32 m0, %1\n\ts_nop 0\n\tglobal_load_lds_dwordx4 %2, %3\n\ts_mov_b32 m0, %0"
               : "=&s"(keep) : "s"(lds_addr), "v"(voff), "s"(base) : "memory");
}

static DI bf16x8 pack8(const f32x16& v, int s) {
  f32x8 t;
#pragma unroll
  for (int j = 0; j < 8; ++j) t[j] = v[8 * s + j];
  return __builtin_convertvector(t, bf16x8);
}

__global__ __launch_bounds__(512, 2) void main_kernel(const u16* __restrict__ x, const u16* __restrict__ ea,
                                                      const u32x2* __restrict__ recbuf, const u32* __restrict__ segT,
                                                      const u32x4* __restrict__ wimg,
                                                      const u16* __restrict__ wa, const u16* __restrict__ wb,
                                                      const u16* __restrict__ wc, const u16* __restrict__ wu,
                                                      const u16* __restrict__ wv,
                                                      u32* __restrict__ out, int N, int CH, int NPB) {
  extern __shared__ __attribute__((aligned(16))) char smem[];
  const int tid = threadIdx.x;
  const int lane = tid & 63;
  const int wid = __builtin_amdgcn_readfirstlane(tid >> 6);
  const int bidx = blockIdx.x;
  const int b = (bidx & 7) * (NBK / 8) + (bidx >> 3);
  const int n0 = b * NPB;
  int nn = N - n0; nn = nn < 0 ? 0 : (nn > NPB ? NPB : nn);
  if (nn == 0) return;

  u32* hist = (u32*)(smem + L_HIST);
  u32* pfx = (u32*)(smem + L_PFX);
  u32* win = (u32*)(smem + L_WIN);
  u32* lsrc = (u32*)(smem + L_SRC);
  u32* leid = (u32*)(smem + L_EID);
  u16* ltof = (u16*)(smem + L_TOF);

  const int hw = tid >> 5, j32 = tid & 31;
  u32 segv = segT[b * G1 + hw * 16 + (j32 & 15)];
#pragma unroll
  for (int r = 0; r < 5; ++r) {
    const int u = tid + 512 * r;
    if (u < WIMG_UNITS) {
      u32x4 v = wimg[u];
      const int off = u < WIMG_WA ? (L_STG + 16 * u) : (L_WAU + 16 * (u - WIMG_WA));
      *(u32x4*)(smem + off) = v;
    }
  }
  for (int i = tid; i < 33 * 16; i += 512) {
    const int d = i >> 4, dw = i & 15;
    u32 v2 = (2 * dw == d ? 0x3F80u : 0u) | (2 * dw + 1 == d ? 0x3F800000u : 0u);
    u32 v1 = 0;
#pragma unroll
    for (int e = 0; e < 2; ++e) {
      const int p = 2 * dw + e, sh = p >> 3, jj = p & 7;
      const int slot = 16 * (sh >> 1) + 8 * (jj >> 2) + 4 * (sh & 1) + (jj & 3);
      if (slot == d) v1 |= 0x3F80u << (16 * e);
    }
    *(u32*)(smem + L_T1 + 4 * i) = v1;
    *(u32*)(smem + L_T2 + 4 * i) = v2;
  }
  if (tid < 256) hist[tid] = 0;
  u32x2 rec[16];
  u32 okmask = 0;
  u32 anybig = 0;
#pragma unroll
  for (int r = 0; r < 16; ++r) {
    const u32 s = __shfl(segv, (lane & 32) + r, 64);
    const u32 off = s >> 16, cnt = s & 0xFFFFu;
    anybig |= cnt > 32u;
    if ((u32)j32 < cnt) {
      rec[r] = recbuf[(size_t)(hw * 16 + r) * CH + off + j32];
      okmask |= 1u << r;
    }
  }
  __syncthreads();
#pragma unroll
  for (int r = 0; r < 16; ++r)
    if (okmask & (1u << r)) atomicAdd(&hist[rec[r][1] & 255u], 1u);
  if (anybig) {
    for (int r = 0; r < 16; ++r) {
      const u32 sg = segT[b * G1 + hw * 16 + r];
      const u32 off = sg >> 16, cnt = sg & 0xFFFFu;
      for (u32 jj = j32 + 32; jj < cnt; jj += 32) {
        u32x2 rr = recbuf[(size_t)(hw * 16 + r) * CH + off + jj];
        atomicAdd(&hist[rr[1] & 255u], 1u);
      }
    }
  }
  bf16x8 fwb[2][4], fwv[2][4], fwc[2];
#pragma unroll
  for (int t = 0; t < 2; ++t) {
#pragma unroll
    for (int s = 0; s < 4; ++s) {
      fwb[t][s] = *(const bf16x8*)(smem + L_STG + 16 * (WIMG_WB + (t * 4 + s) * 64 + lane));
      fwv[t][s] = *(const bf16x8*)(smem + L_STG + 16 * (WIMG_WV + (t * 4 + s) * 64 + lane));
    }
    fwc[t] = *(const bf16x8*)(smem + L_STG + 16 * (WIMG_WC + t * 64 + lane));
  }
  __syncthreads();
  if (wid == 0) {
    u32x4 h4 = *(const u32x4*)&hist[4 * lane];
    u32 tot = h4[0] + h4[1] + h4[2] + h4[3];
    u32 inc = tot;
#pragma unroll
    for (int o = 1; o < 64; o <<= 1) { u32 y = __shfl_up(inc, o, 64); if (lane >= o) inc += y; }
    const u32 etot = __builtin_amdgcn_readlane(inc, 63);
    u32 p[4];
    p[0] = inc - tot; p[1] = p[0] + h4[0]; p[2] = p[1] + h4[1]; p[3] = p[2] + h4[2];
    u32x4 p4 = {p[0], p[1], p[2], p[3]};
    *(u32x4*)&pfx[4 * lane] = p4;
    if (lane == 0) pfx[256] = etot;
    int sw[9];
    sw[0] = 0; sw[8] = nn;
#pragma unroll
    for (int w = 1; w < 8; ++w) {
      const u32 tw = (etot * (u32)w) >> 3;
      int raw = 0;
#pragma unroll
      for (int j = 0; j < 4; ++j) raw += __popcll(__ballot(p[j] < tw && 4 * lane + j < nn));
      int lo = nn - 32 * (8 - w); lo = lo > sw[w - 1] ? lo : sw[w - 1];
      int hi = sw[w - 1] + 32; hi = hi < nn ? hi : nn;
      raw = raw < lo ? lo : raw; raw = raw > hi ? hi : raw;
      sw[w] = raw;
    }
    u32 pw[9];
#pragma unroll
    for (int w = 0; w < 9; ++w) pw[w] = __builtin_amdgcn_readfirstlane(pfx[sw[w] < 256 ? sw[w] : 256]);
    pw[8] = etot;
    u32 lw[9];
    lw[0] = 0;
#pragma unroll
    for (int w = 0; w < 8; ++w) lw[w + 1] = lw[w] + ((pw[w + 1] - pw[w] + 31u) & ~31u);
    u32 cur[4];
#pragma unroll
    for (int j = 0; j < 4; ++j) {
      const int n = 4 * lane + j;
      int sb = sw[0]; u32 adj = lw[0] - pw[0];
#pragma unroll
      for (int w = 1; w < 8; ++w) { if (n >= sw[w]) { sb = sw[w]; adj = lw[w] - pw[w]; } }
      cur[j] = ((u32)(n - sb) << 16) + (p[j] + adj);
    }
    u32x4 c4 = {cur[0], cur[1], cur[2], cur[3]};
    *(u32x4*)&hist[4 * lane] = c4;
    if (lane < 8) {
      int a = sw[0], e = sw[1]; u32 l0 = lw[0], ec = pw[1] - pw[0];
#pragma unroll
      for (int w = 1; w < 8; ++w) if (lane == w) { a = sw[w]; e = sw[w + 1]; l0 = lw[w]; ec = pw[w + 1] - pw[w]; }
      u32x4 wv4 = {(u32)a, (u32)(e - a), l0, ec};
      *(u32x4*)&win[4 * lane] = wv4;
    }
    if (lane == 0) win[32] = lw[8] > (u32)CAPP ? 1u : 0u;
  }
  __syncthreads();
  if (win[32]) {
    float* acc = (float*)smem;
    __syncthreads();
    for (int i = tid; i < nn * 128; i += 512) acc[i] = 0.f;
    __syncthreads();
    for (int c = 0; c < G1; ++c) {
      const u32 sg = segT[b * G1 + c];
      const u32 off = sg >> 16, cnt = sg & 0xFFFFu;
      for (u32 k = wid; k < cnt; k += 8) {
        const u32x2 rr = recbuf[(size_t)c * CH + off + k];
        const u32 s = rr[0], e = rr[1] >> 8, dl = rr[1] & 255u;
        const u16* xs = x + (size_t)s * 64;
        const u16* xd = x + (size_t)(n0 + dl) * 64;
        float g = 0.f, hv = 0.f;
        for (int kk = 0; kk < 64; ++kk) {
          const float a = bf2f(xs[kk]);
          g += bf2f(xd[kk]) * bf2f(wa[kk * 64 + lane]) + a * bf2f(wb[kk * 64 + lane]);
          hv += a * bf2f(wv[kk * 64 + lane]);
        }
        for (int kk = 0; kk < 16; ++kk) g += bf2f(ea[(size_t)e * 16 + kk]) * bf2f(wc[kk * 64 + lane]);
        const float eta = 1.f / (1.f + __expf(-g));
        atomicAdd(&acc[dl * 128 + lane], eta);
        atomicAdd(&acc[dl * 128 + 64 + lane], eta * hv);
      }
    }
    __syncthreads();
    for (int i = tid; i < nn * 32; i += 512) {
      const int n = i >> 5, c2 = i & 31;
      const u16* xr = x + (size_t)(n0 + n) * 64;
      float o[2];
      for (int t = 0; t < 2; ++t) {
        const int f = 2 * c2 + t;
        float z = 0.f;
        for (int kk = 0; kk < 64; ++kk) z += bf2f(xr[kk]) * bf2f(wu[kk * 64 + f]);
        z += acc[n * 128 + 64 + f] / (acc[n * 128 + f] + 1e-6f);
        o[t] = bf2f(xr[f]) + (z > 0.f ? z : 0.f);
      }
      f32x2 ov = {o[0], o[1]};
      out[(size_t)(n0 + n) * 32 + c2] = __builtin_bit_cast(u32, __builtin_convertvector(ov, bf16x2));
    }
    return;
  }
  const int wbase = __builtin_amdgcn_readfirstlane(win[4 * wid]);
  const int wcnt = __builtin_amdgcn_readfirstlane(win[4 * wid + 1]);
  const int lbase = __builtin_amdgcn_readfirstlane(win[4 * wid + 2]);
  const int ecnt = __builtin_amdgcn_readfirstlane(win[4 * wid + 3]);
  const int r32 = lane & 31, h = lane >> 5;
  int xrow = n0 + wbase + r32; xrow = xrow < N ? xrow : N - 1;
  bf16x8 xw[4];
#pragma unroll
  for (int s = 0; s < 4; ++s) xw[s] = *(const bf16x8*)(x + (size_t)xrow * 64 + 16 * s + 8 * h);
#pragma unroll
  for (int r = 0; r < 16; ++r) {
    if (okmask & (1u << r)) {
      const u32 cv = atomicAdd(&hist[rec[r][1] & 255u], 1u);
      const u32 pos = cv & 0xFFFFu;
      lsrc[pos] = rec[r][0] << 7;
      leid[pos] = (rec[r][1] >> 8) << 5;
      ltof[pos] = (u16)((cv >> 16) << 6);
    }
  }
  if (anybig) {
    for (int r = 0; r < 16; ++r) {
      const u32 sg = segT[b * G1 + hw * 16 + r];
      const u32 off = sg >> 16, cnt = sg & 0xFFFFu;
      for (u32 jj = j32 + 32; jj < cnt; jj += 32) {
        u32x2 rr = recbuf[(size_t)(hw * 16 + r) * CH + off + jj];
        const u32 cv = atomicAdd(&hist[rr[1] & 255u], 1u);
        const u32 pos = cv & 0xFFFFu;
        lsrc[pos] = rr[0] << 7;
        leid[pos] = (rr[1] >> 8) << 5;
        ltof[pos] = (u16)((cv >> 16) << 6);
      }
    }
  }
  const int ntile = (ecnt + 31) >> 5;
  {
    const int p = ecnt + lane;
    if (lane < 32 && p < ntile * 32) { lsrc[lbase + p] = 0; leid[lbase + p] = 0; ltof[lbase + p] = (u16)(32 << 6); }
  }
  bf16x8 fxa[2][2];
  {
#pragma unroll
    for (int t = 0; t < 2; ++t) {
      f32x16 a = {};
#pragma unroll
      for (int s = 0; s < 4; ++s) {
        bf16x8 wf = *(const bf16x8*)(smem + L_WAU + 16 * ((t * 4 + s) * 64 + lane));
        a = MFMA32(xw[s], wf, a);
      }
      fxa[t][0] = pack8(a, 0);
      fxa[t][1] = pack8(a, 1);
    }
  }
  __syncthreads();

  f32x16 yd[2] = {}, yn[2] = {};
  const u32 stg = (u32)(L_STG + wid * STG_WAVE);
  const u32 ldsbase = (u32)(size_t)smem;
  const int q8 = lane >> 3, p8 = lane & 7;
  const u32 xo0 = 16u * (u32)(p8 ^ (q8 >> 1));
  const u32 xo1 = xo0 ^ 64u;
  const u32 eo = 16u * (u32)(lane & 1);
  const int ls_i = 4 * q8;
  const int le_i = ((lane >> 1) & 7) * 4 + (lane >> 4);
  const int lt1_i = (r32 & 7) * 4 + (r32 >> 3);
  const int lt2_i = (4 * h + ((lane >> 2) & 3)) * 4;
  const u32 t1o = (u32)(L_T1 + 16 * h);
  const u32 t2o = (u32)(L_T2 + (16 * ((lane >> 4) & 1) + 4 * (lane & 3)) * 2);
  u32 ao[4];
#pragma unroll
  for (int s = 0; s < 4; ++s) ao[s] = (u32)(128 * r32 + 16 * ((2 * s + h) ^ ((r32 >> 1) & 7)));
  const u32 aeo = (u32)(4096 + 32 * r32 + 16 * h);

  auto issue = [&](int t) {
    const int base = lbase + 32 * t;
    const u32x4 sv = *(const u32x4*)&lsrc[base + ls_i];
    const u32 ev = leid[base + le_i];
    const u32 bufo = ldsbase + stg + (u32)((t & 1) * STG_BUF);
    dma16(bufo + 0, sv[0] + xo0, x);
    dma16(bufo + 1024, sv[1] + xo1, x);
    dma16(bufo + 2048, sv[2] + xo0, x);
    dma16(bufo + 3072, sv[3] + xo1, x);
    dma16(bufo + 4096, ev + eo, ea);
  };
  if (ntile > 0) issue(0);
  if (ntile > 1) issue(1);
  for (int t = 0; t < ntile; ++t) {
    const int base = lbase + 32 * t;
    const u32 tf1 = ltof[base + lt1_i];
    const i16x4 tf2 = *(const i16x4*)&ltof[base + lt2_i];
    if (t + 1 < ntile) asm volatile("s_waitcnt vmcnt(5)" ::: "memory");
    else asm volatile("s_waitcnt vmcnt(0)" ::: "memory");
    const char* buf = smem + stg + (t & 1) * STG_BUF;
    bf16x8 ax[4];
#pragma unroll
    for (int s = 0; s < 4; ++s) ax[s] = *(const bf16x8*)(buf + ao[s]);
    const bf16x8 ae = *(const bf16x8*)(buf + aeo);
    bf16x8 st[2];
    st[0] = *(const bf16x8*)(smem + t1o + tf1);
    st[1] = *(const bf16x8*)(smem + t1o + tf1 + 32);
    bf16x8 sa[2];
    {
      typedef __attribute__((address_space(3))) i16x4* lp;
      i16x4 q0 = __builtin_amdgcn_ds_read_tr16_b64_v4i16((lp)(smem + t2o + (u16)tf2[0]));
      i16x4 q1 = __builtin_amdgcn_ds_read_tr16_b64_v4i16((lp)(smem + t2o + (u16)tf2[1]));
      i16x4 q2 = __builtin_amdgcn_ds_read_tr16_b64_v4i16((lp)(smem + t2o + (u16)tf2[2]));
      i16x4 q3 = __builtin_amdgcn_ds_read_tr16_b64_v4i16((lp)(smem + t2o + (u16)tf2[3]));
      sa[0] = __builtin_bit_cast(bf16x8, __builtin_shufflevector(q0, q1, 0, 1, 2, 3, 4, 5, 6, 7));
      sa[1] = __builtin_bit_cast(bf16x8, __builtin_shufflevector(q2, q3, 0, 1, 2, 3, 4, 5, 6, 7));
    }
    asm volatile("s_waitcnt lgkmcnt(0)" : "+v"(ax[0]), "+v"(ax[1]), "+v"(ax[2]), "+v"(ax[3]) :: "memory");
    if (t + 2 < ntile) issue(t + 2);
#pragma unroll
    for (int c = 0; c < 2; ++c) {
      f32x16 g = {};
#pragma unroll
      for (int s = 0; s < 4; ++s) g = MFMA32(ax[s], fwb[c][s], g);
      g = MFMA32(ae, fwc[c], g);
      g = MFMA32(st[0], fxa[c][0], g);
      g = MFMA32(st[1], fxa[c][1], g);
      f32x16 hv = {};
#pragma unroll
      for (int s = 0; s < 4; ++s) hv = MFMA32(ax[s], fwv[c][s], hv);
      f32x16 eta, pr;
#pragma unroll
      for (int i = 0; i < 16; ++i) {
        const float e = __builtin_amdgcn_exp2f(g[i] * -1.4426950408889634f);
        eta[i] = __builtin_amdgcn_rcpf(1.0f + e);
        pr[i] = eta[i] * hv[i];
      }
      yd[c] = MFMA32(sa[0], pack8(eta, 0), yd[c]);
      yd[c] = MFMA32(sa[1], pack8(eta, 1), yd[c]);
      yn[c] = MFMA32(sa[0], pack8(pr, 0), yn[c]);
      yn[c] = MFMA32(sa[1], pack8(pr, 1), yn[c]);
    }
  }
  {
#pragma unroll
    for (int s = 0; s < 4; ++s) xw[s] = *(const bf16x8*)(x + (size_t)xrow * 64 + 16 * s + 8 * h);
    const u32* x32 = (const u32*)x;
    u32 xv[16];
#pragma unroll
    for (int i = 0; i < 16; ++i) {
      const int slot = (i & 3) + 8 * (i >> 2) + 4 * h;
      int node = n0 + wbase + slot; node = node < N ? node : N - 1;
      xv[i] = x32[(size_t)node * 32 + r32];
    }
    f32x16 zu[2];
#pragma unroll
    for (int t = 0; t < 2; ++t) {
      f32x16 a = {};
#pragma unroll
      for (int s = 0; s < 4; ++s) {
        bf16x8 wf = *(const bf16x8*)(smem + L_WAU + 8192 + 16 * ((t * 4 + s) * 64 + lane));
        a = MFMA32(xw[s], wf, a);
      }
      zu[t] = a;
    }
#pragma unroll
    for (int i = 0; i < 16; ++i) {
      const int slot = (i & 3) + 8 * (i >> 2) + 4 * h;
      float z0 = zu[0][i] + yn[0][i] * __builtin_amdgcn_rcpf(yd[0][i] + 1e-6f);
      float z1 = zu[1][i] + yn[1][i] * __builtin_amdgcn_rcpf(yd[1][i] + 1e-6f);
      f32x2 o;
      o[0] = __uint_as_float(xv[i] << 16) + fmaxf(z0, 0.f);
      o[1] = __uint_as_float(xv[i] & 0xFFFF0000u) + fmaxf(z1, 0.f);
      if (slot < wcnt) out[(size_t)(n0 + wbase + slot) * 32 + r32] = __builtin_bit_cast(u32, __builtin_convertvector(o, bf16x2));
    }
  }
}

__global__ void fb_proj_kernel(const u16* x, const u16* wa, const u16* wb, const u16* wv, const u16* wu, float* proj,
                               float* acc, int N) {
  int n = blockIdx.x;
  int t = threadIdx.x;
  int m = t >> 6, f = t & 63;
  const u16* w = m == 0 ? wa : m == 1 ? wb : m == 2 ? wv : wu;
  float s = 0.f;
  for (int k = 0; k < 64; ++k) s += bf2f(x[(size_t)n * 64 + k]) * bf2f(w[k * 64 + f]);
  proj[(size_t)n * 256 + t] = s;
  if (t < 128) acc[(size_t)n * 128 + t] = 0.f;
}
__global__ void fb_edge_kernel(const u16* ea, const int* src, const int* dst, const u16* wc, const float* proj, float* acc,
                               int E) {
  int e = blockIdx.x * 4 + (threadIdx.x >> 6);
  int f = threadIdx.x & 63;
  if (e >= E) return;
  int s = src[e], d = dst[e];
  float g = proj[(size_t)d * 256 + f] + proj[(size_t)s * 256 + 64 + f];
  for (int k = 0; k < 16; ++k) g += bf2f(ea[(size_t)e * 16 + k]) * bf2f(wc[k * 64 + f]);
  float eta = 1.f / (1.f + __expf(-g));
  atomicAdd(&acc[(size_t)d * 128 + f], eta);
  atomicAdd(&acc[(size_t)d * 128 + 64 + f], eta * proj[(size_t)s * 256 + 128 + f]);
}
__global__ void fb_fin_kernel(const u16* x, const float* proj, const float* acc, __hip_bfloat16* out, int N) {
  size_t i = (size_t)blockIdx.x * 256 + threadIdx.x;
  if (i >= (size_t)N * 64) return;
  size_t n = i >> 6, f = i & 63;
  float agg = acc[n * 128 + 64 + f] / (acc[n * 128 + f] + 1e-6f);
  float z = proj[n * 256 + 192 + f] + agg;
  out[i] = __float2bfloat16(bf2f(x[i]) + (z > 0.f ? z : 0.f));
}

extern "C" void kernel_launch(void* const* d_in, const int* in_sizes, int n_in, void* d_out, int out_size, void* d_ws,
                              size_t ws_size, hipStream_t stream) {
  const u16* x = (const u16*)d_in[0];
  const u16* ea = (const u16*)d_in[1];
  const int* src = (const int*)d_in[2];
  const int* dst = (const int*)d_in[3];
  const u16* wa = (const u16*)d_in[4];
  const u16* wb = (const u16*)d_in[5];
  const u16* wc = (const u16*)d_in[6];
  const u16* wu = (const u16*)d_in[7];
  const u16* wv = (const u16*)d_in[8];
  const int N = in_sizes[0] / 64, E = in_sizes[2];
  const int NPB = (N + NBK - 1) / NBK;
  int CH = (E + G1 - 1) / G1; CH = (CH + 3) & ~3;
  const bool fast = N >= 1 && NPB <= 256 && NPB >= 2 && CH <= CHMAX && CH >= 4 && E < (1 << 24) && N <= (1 << 24) &&
                    (((uintptr_t)src | (uintptr_t)dst | (uintptr_t)x | (uintptr_t)ea) & 15) == 0;
  if (!fast) {
    float* proj = (float*)d_ws;
    float* acc = proj + (size_t)N * 256;
    if (N > 0) fb_proj_kernel<<<N, 256, 0, stream>>>(x, wa, wb, wv, wu, proj, acc, N);
    if (E > 0) fb_edge_kernel<<<(E + 3) / 4, 256, 0, stream>>>(ea, src, dst, wc, proj, acc, E);
    if (N > 0) fb_fin_kernel<<<(N * 64 + 255) / 256, 256, 0, stream>>>(x, proj, acc, (__hip_bfloat16*)d_out, N);
    return;
  }
  const u32 magic = (u32)(((1ull << 32) + (u32)NPB - 1) / (u32)NPB);
  char* ws = (char*)d_ws;
  u32x2* recbuf = (u32x2*)ws;
  u32* segT = (u32*)(ws + (size_t)G1 * CHMAX * 8);
  u32x4* wimg = (u32x4*)(ws + (size_t)G1 * CHMAX * 8 + NBK * G1 * 4);
  part_kernel<<<G1 + 3, 1024, 0, stream>>>(src, dst, recbuf, segT, wa, wb, wc, wu, wv, wimg, E, CH, NPB, magic);
  (void)hipFuncSetAttribute((const void*)main_kernel, hipFuncAttributeMaxDynamicSharedMemorySize, L_TOTAL);
  main_kernel<<<NBK, 512, L_TOTAL, stream>>>(x, ea, recbuf, segT, wimg, wa, wb, wc, wu, wv, (u32*)d_out, N, CH, NPB);
}
```

```cpp
#define DELAY 330
#define NPF 1
#define PFW 4
#include <hip/hip_runtime.h>
#include <hip/hip_bf16.h>
#include <stdint.h>
#include <string.h>

typedef unsigned int u32;
typedef unsigned short u16;
typedef unsigned long long u64;
using bf16x8 = __attribute__((ext_vector_type(8))) __bf16;
using i16x4 = __attribute__((ext_vector_type(4))) short;
using f32x16 = __attribute__((ext_vector_type(16))) float;
using f32x8 = __attribute__((ext_vector_type(8))) float;
using f32x2 = __attribute__((ext_vector_type(2))) float;
using bf16x2 = __attribute__((ext_vector_type(2))) __bf16;
using u32x4 = __attribute__((ext_vector_type(4))) u32;
using u32x2 = __attribute__((ext_vector_type(2))) u32;
using u32x3 = __attribute__((ext_vector_type(3))) u32;
using u32x8 = __attribute__((ext_vector_type(8))) u32;
using u32x16 = __attribute__((ext_vector_type(16))) u32;

#if defined(__HIP_DEVICE_COMPILE__)
#define NOPK __attribute__((target("no-packed-fp32-ops")))
#else
#define NOPK
#endif
#define DI __device__ __forceinline__ NOPK
#define BAR() do { __builtin_amdgcn_fence(__ATOMIC_RELEASE, "workgroup"); __builtin_amdgcn_s_barrier(); __builtin_amdgcn_fence(__ATOMIC_ACQUIRE, "workgroup"); } while (0)
#define SB() __builtin_amdgcn_sched_barrier(0)
#define LDS_ADD(p, v) __hip_atomic_fetch_add((p), (v), __ATOMIC_RELAXED, __HIP_MEMORY_SCOPE_WORKGROUP)
#define U2F(u) __builtin_bit_cast(float, (u32)(u))
#define MFMA32(a, b, c) __builtin_amdgcn_mfma_f32_32x32x16_bf16((a), (b), (c), 0, 0, 0)

constexpr int NBK = 256;
constexpr int G1 = 256;
constexpr int CHMAX = 4096;
constexpr int WNMAX = 25;
constexpr int CW = 664;
constexpr int CA = 268;
constexpr int SCAP = 24;
#define K1LP "nt"
#define RECP "sc1"
#define XBP ""
#define CPF 8
#define K1AUX 17
#define WUAUX 0
#define OUTAUX 17
constexpr u64 SPIN_LIMIT = 200000;

constexpr u32 W_P1 = NBK * G1 * 64, W_WU = 2 * W_P1, W_SEC = W_WU + NBK * 8192, W_END = W_SEC + NBK * G1 * 512;

constexpr int I_WB = 0, I_WA = 512, I_WC = 1024, I_WV = 1152;

constexpr int STG_BUF = 40960, STG_WAVE = 5120;
constexpr int L_T1 = 0, L_T2 = 2048;
constexpr int L_CNT = 4096;
constexpr int L_STG = 4608;
constexpr int L_SEG = L_STG, L_HIST = L_STG + 65536;
constexpr int L_WV = L_STG + 81920;
constexpr int L_IMG = L_WV - 16 * I_WV;
constexpr int L_SRC = L_WV + 8192;
constexpr int L_EID = L_SRC + 4 * 8 * CW;
constexpr int L_XB = L_EID + 4 * 8 * CW;
constexpr int L_TOTAL = L_XB + (7 * WNMAX + 33) * 128;
static_assert(L_EID - L_SRC == 83 * 256, "write2st64 offset");
static_assert(L_TOTAL <= 163840, "LDS");

static DI float bf2f(u16 u) { return U2F(((u32)u) << 16); }
static __device__ __forceinline__ float bf2g(u16 u) { return U2F(((u32)u) << 16); }

static DI u32 madlo128(u32 a, u32 c) { u32 t; asm("v_mad_u32_u16 %0, %1, %2, %3" : "=v"(t) : "v"(a), "s"(128u), "v"(c)); return t; }
static DI u32 cvtpk(float a, float b) { f32x2 v = {a, b}; return __builtin_bit_cast(u32, __builtin_convertvector(v, bf16x2)); }
static DI bf16x8 pack8(const f32x16& v, int s) {
  f32x8 t;
#pragma unroll
  for (int j = 0; j < 8; ++j) t[j] = v[8 * s + j];
  return __builtin_convertvector(t, bf16x8);
}

extern "C" __device__ u64 gnn_dispatch_id() __asm("llvm.amdgcn.dispatch.id");

__global__ __launch_bounds__(512, 2) NOPK void main_kernel(u32x4* __restrict__ prim0, const u16* __restrict__ x,
                                                           const u16* __restrict__ ea, u32* __restrict__ out,
                                                           int N, int CH, int H, int E,
                                                           const int* __restrict__ src_, const int* __restrict__ dst_,
                                                           const u16* __restrict__ wa_, const u16* __restrict__ wb_,
                                                           const u16* __restrict__ wc_, const u16* __restrict__ wu_,
                                                           const u16* __restrict__ wv_, u32 magw, int unused
  ) {
  extern __shared__ __attribute__((aligned(16))) char smem[];
  asm volatile("" :: "s"(x), "s"(ea), "s"(prim0), "s"(out), "s"(N), "s"(CH), "s"(H), "s"(E), "s"(src_), "s"(dst_), "s"(wa_), "s"(wb_), "s"(wc_), "s"(wu_), "s"(wv_), "s"(magw));
  const int tid = (int)__builtin_amdgcn_workitem_id_x();
  const int lane = tid & 63;
  const int wid = __builtin_amdgcn_readfirstlane(tid >> 6);
  const int bidx = (int)__builtin_amdgcn_workgroup_id_x();
  const int b = bidx;
  const int ebase = b * CH;
  int cnt = E - ebase; cnt = cnt < 0 ? 0 : (cnt > CH ? CH : cnt);
  const int i0 = tid * 4, i1 = i0 + 2048;
  const bool act0 = i0 + 4 <= cnt, act1 = i1 + 4 <= cnt;
  u32x4 dv0, sv0, dv1, sv1;
  if (wid == 7 && b < CPF) {
    u64 pc; u32 o0, o1;
    asm volatile("s_mov_b32 m0, %4\n\ts_getpc_b64 %0\ngnn_here:\n\tv_add_u32 %1, gnn_pf-gnn_here, %3\n\tv_add_u32 %2, gnn_pf-gnn_here+8192, %3\n\t"
                 "global_load_lds_dword %1, %0\n\tglobal_load_lds_dword %2, %0"
                 : "=&s"(pc), "=&v"(o0), "=&v"(o1) : "v"(128u * (u32)lane), "s"((u32)(size_t)smem + L_CNT + 256) : "memory");
  }
#define EDGE_LOAD(dv, sv, i) asm volatile("global_load_dwordx4 %0, %2, %3 " K1LP "\n\tglobal_load_dwordx4 %1, %2, %4 " K1LP : "=&v"(dv), "=&v"(sv) : "v"(4u * (u32)(ebase + (i))), "s"(dst_), "s"(src_) : "memory")
  if (act1) EDGE_LOAD(dv1, sv1, i1);
  if (act0) EDGE_LOAD(dv0, sv0, i0);
  SB();
  {
    const u16* pA = wid < 4 ? wb_ : wa_; const u16* pB = wid < 4 ? wv_ : wu_;
    const u16* pC = (wid & 1) ? pB : pA; const u16* pw = (wid & 2) ? wc_ : pC;
    const u32 ow = ((((u32)b >> 3) & 31u) * 256u + 128u * (u32)lane) & ((wid & 2) ? 2047u : 8191u);
    asm volatile("s_mov_b32 m0, %2\n\ts_mov_b64 exec, 3\n\tglobal_load_lds_dword %0, %1\n\ts_mov_b64 exec, -1" :: "v"(ow), "s"(pw), "s"((u32)(size_t)smem + L_CNT + 256) : "memory");
  }
  const int r32 = lane & 31, h = lane >> 5;
  const int q8 = lane >> 3, p8 = lane & 7;
  u32 wA[8], wB[8], wC;
#define W_LOAD() do { \
    const u32 oA = 2u * (u32)(1024 * (wid & 3) + 512 * h + 2 * r32); \
    const u16* pA = wid < 4 ? wb_ : wa_; const u16* pB = wid < 4 ? wv_ : wu_; \
    _Pragma("unroll") for (int k = 0; k < 8; ++k) asm volatile("global_load_dword %0, %1, %2 offset:%3" : "=v"(wA[k]) : "v"(oA), "s"(pA), "n"(128 * k) : "memory"); \
    _Pragma("unroll") for (int k = 0; k < 8; ++k) asm volatile("global_load_dword %0, %1, %2 offset:%3" : "=v"(wB[k]) : "v"(oA), "s"(pB), "n"(128 * k) : "memory"); \
    asm volatile("global_load_dword %0, %1, %2" : "=v"(wC) : "v"(2u * (u32)(512 * h + 64 * wid + 2 * r32)), "s"(wc_) : "memory"); } while (0)
#define W_WAIT(n) asm volatile("s_waitcnt vmcnt(%17)" : "+v"(wA[0]), "+v"(wA[1]), "+v"(wA[2]), "+v"(wA[3]), "+v"(wA[4]), "+v"(wA[5]), "+v"(wA[6]), "+v"(wA[7]), "+v"(wC), \
               "+v"(wB[0]), "+v"(wB[1]), "+v"(wB[2]), "+v"(wB[3]), "+v"(wB[4]), "+v"(wB[5]), "+v"(wB[6]), "+v"(wB[7]) : "n"(n) : "memory")
  u64 tagx;
  {
    const u64 did = gnn_dispatch_id(), ring = (u64)__builtin_amdgcn_dispatch_ptr() >> 16;
    u64 hx = ((did >> 16) ^ (ring * 0x9E3779B97F4A7C15ull)) * 0xBF58476D1CE4E5B9ull;
    hx = (hx ^ (hx >> 29)) * 0x94D049BB133111EBull;
    tagx = (hx ^ (hx >> 32)) ^ ((did & 0xFFFFull) * 0x0001000100010001ull);
  }
  const u32 ql = (u32)lane & 3u;
  u32 ntag = (u32)(tagx >> (16u * ql)) << 16;
  const u64 t_start = __builtin_amdgcn_s_memrealtime();
  const __amdgpu_buffer_rsrc_t rs = __builtin_amdgcn_make_buffer_rsrc((void*)prim0, 0, W_END, 0x00020000);
  u32x4 rec[4];
  const u32 part = ((u32)lane >> 4) & 1u;
  const u32 sga = 8u * (u32)wid + (((u32)lane >> 5) << 2) + (((u32)lane >> 2) & 3u);
  const u32 sg0 = sga + 192u * part, sg1 = sga + 64u + 64u * part;
  u32 ro0 = (u32)b * 16384u + sg0 * 64u + ql * 16u, ro1 = (u32)b * 16384u + sg1 * 64u + ql * 16u;
  const void* prim1 = (const char*)prim0 + W_P1;
  u32x4 nx[4];
#define REC_LOAD(p0, p1, p2, p3) \
  asm volatile("s_mov_b64 exec, %8\n\tglobal_load_dwordx4 %0, %4, %6 " RECP "\n\ts_mov_b64 exec, %9\n\tglobal_load_dwordx4 %1, %5, %6 " RECP "\n\t" \
               "s_mov_b64 exec, %10\n\tglobal_load_dwordx4 %2, %4, %7 " RECP "\n\ts_mov_b64 exec, %11\n\tglobal_load_dwordx4 %3, %5, %7 " RECP "\n\ts_mov_b64 exec, -1" \
               : "=&{v[200:203]}"(nx[0]), "=&{v[204:207]}"(nx[1]), "=&{v[208:211]}"(nx[2]), "=&{v[212:215]}"(nx[3]) \
               : "v"(ro0), "v"(ro1), "s"(prim0), "s"(prim1), "s"(p0), "s"(p1), "s"(p2), "s"(p3) : "memory")
#define REC_TAKE() \
  asm volatile("s_waitcnt vmcnt(0)\n\t" \
               "v_mov_b64 v[176:177], v[200:201]\n\tv_mov_b64 v[178:179], v[202:203]\n\tv_mov_b64 v[180:181], v[204:205]\n\tv_mov_b64 v[182:183], v[206:207]\n\t" \
               "v_mov_b64 v[184:185], v[208:209]\n\tv_mov_b64 v[186:187], v[210:211]\n\tv_mov_b64 v[188:189], v[212:213]\n\tv_mov_b64 v[190:191], v[214:215]" \
               : "=&{v[176:179]}"(rec[0]), "=&{v[180:183]}"(rec[1]), "=&{v[184:187]}"(rec[2]), "=&{v[188:191]}"(rec[3]) \
               : "{v[200:203]}"(nx[0]), "{v[204:207]}"(nx[1]), "{v[208:211]}"(nx[2]), "{v[212:215]}"(nx[3]) : "memory")
#define WSTART(g) __builtin_amdgcn_readfirstlane(((g) * N) >> 11)
  const u32 l16 = 16u * (u32)lane;
  const int n0 = WSTART(8 * b), wn0 = WSTART(8 * b + wid);
  const u32 ldsbase = (u32)(size_t)smem;
  u32* cntp = (u32*)(smem + L_CNT);
  const void* xs = (const char*)x - (size_t)(32768 - H) * 128;
  u32 cb0 = (sg0 * (u32)CH) << 11, cb1 = (sg1 * (u32)CH) << 11;
  const u32 part4 = part << 2;
  const u32 lsb = ldsbase + L_SRC;
  const u32 four = part ? 4u : ~3u;
#define BC8(v) __builtin_bit_cast(bf16x8, (v))
  const u32 xo0 = 16u * (u32)(p8 ^ (q8 >> 1));
  u32x4 lp = {16u * (u32)q8, (u32)(L_EID - L_SRC) + 4u * (u32)(((lane >> 1) & 7) * 4 + (lane >> 4)),
              (u32)(L_EID - L_SRC) + 4u * (u32)((r32 & 7) * 4 + (r32 >> 3)), (u32)(L_EID - L_SRC) + 16u * (u32)(4 * h + ((lane >> 2) & 3))};
  const u32 stg = ldsbase + (u32)(L_STG + wid * STG_WAVE);
  u32x4 aov, mi1 = {stg + (u32)(4096 + 32 * r32 + 16 * h), ldsbase + (u32)L_WV + l16, ldsbase + (u32)(L_T1 + 16 * h),
                    ldsbase + (u32)((16 * ((lane >> 4) & 1) + 4 * (lane & 3)) * 2)};
#pragma unroll
  for (int s = 0; s < 4; ++s) aov[s] = stg + (u32)(128 * r32 + 16 * ((2 * s + h) ^ ((r32 >> 1) & 7)));
  u32x4 mi2 = {xo0 + 1024u, xo0 ^ 64u, 16u * (u32)(lane & 1), (u32)((lane & 32) + (r32 >> 2) + 8 * (lane & 3)) << 16};
  u32x4 xba;
#pragma unroll
  for (int i = 0; i < 4; ++i) {
    const int row = wn0 - n0 + 8 * i + q8;
    int node = n0 + row; node = node < N ? node : N - 1;
    xba[i] = ((u32)node << 7) + 16u * (u32)(p8 ^ ((row >> 1) & 7));
  }
#define XB_LOAD() asm volatile("s_mov_b32 m0, %4\n\ts_nop 0\n\tglobal_load_lds_dwordx4 %0, %5 " XBP "\n\ts_add_u32 m0, %4, 1024\n\ts_nop 0\n\tglobal_load_lds_dwordx4 %1, %5 " XBP "\n\t" \
    "s_add_u32 m0, %4, 2048\n\ts_nop 0\n\tglobal_load_lds_dwordx4 %2, %5 " XBP "\n\ts_add_u32 m0, %4, 3072\n\ts_nop 0\n\tglobal_load_lds_dwordx4 %3, %5 " XBP \
    :: "v"(xba[0]), "v"(xba[1]), "v"(xba[2]), "v"(xba[3]), "s"(ldsbase + (u32)(L_XB + 128 * (wn0 - n0))), "s"(x) : "memory", "scc")
  u32 cav = 4u * CA, relA, relB, keyi, keyj, basei = lsb + 4u * (u32)(q8 * CW);
  const void* xs1 = (const char*)xs - 1024;
  const void* wu1 = (const char*)prim0 + (W_WU - 1024u + 8192u * (u32)b);
#define SLOT_A(k, i, j) \
    const u32 m##k = rec[i][j]; const u64 mk##k = __builtin_amdgcn_ballot_w64(((i) >> 1) * 3 + (j) < qn[i]) & nv[i]; u32 pos##k; \
    asm volatile("s_mov_b64 exec, %3\n\tds_add_rtn_u32 %0, %1, %2 offset:%4\n\ts_mov_b64 exec, -1" : "=&v"(pos##k) \
                 : "v"((__builtin_amdgcn_ubfe(rec[i][3], 4 * (j), 3) << 3) | part4), "v"(four), "s"(mk##k), "n"(L_CNT) : "memory"); \
    const u32 le##k = madlo128(m##k, (__builtin_amdgcn_ubfe(rec[i][3], 4 * (j) + 3, 1) << 6) | (((i) & 1) ? cb1 : cb0));
#define SLOT_B(k) \
      asm volatile("s_mov_b64 exec, %3\n\tds_write2st64_b32 %0, %1, %2 offset1:83\n\ts_mov_b64 exec, -1" \
                   :: "v"(pos##k), "v"(m##k), "v"(le##k), "s"(mk##k) : "memory")
#define PREFETCH() do { _Pragma("unroll") for (int k = 0; k < NPF; ++k) { \
      const u64 pm_ = __builtin_amdgcn_ballot_w64(k < qn[0] && part == 0u && wid >= PFW) & nv[0]; u32 t0_, t1_; \
      asm volatile("s_mov_b32 m0, %2\n\ts_mov_b64 exec, %7\n\tv_mad_u32_u16 %0, %3, %8, 0 op_sel:[1,0,0,0]\n\tv_lshrrev_b32 %1, 11, %4\n\tv_lshlrev_b32 %1, 5, %1\n\t" \
                   "global_load_lds_dword %0, %5\n\tglobal_load_lds_dword %1, %6\n\ts_mov_b64 exec, -1" \
                   : "=&v"(t0_), "=&v"(t1_) : "s"(ldsbase + L_CNT + 256), "v"(rec[0][k]), "v"(madlo128(rec[0][k], cb0)), "s"(xs), "s"(ea), "s"(pm_), "s"(128u) : "memory"); } } while (0)
#define GIVE_UP() do { cntp[16] = 1u; __hip_atomic_fetch_or(&cntp[1], 0x40000000u, __ATOMIC_RELAXED, __HIP_MEMORY_SCOPE_WORKGROUP); } while (0)
#define TIMED_OUT() (__builtin_amdgcn_s_memrealtime() - t_start > SPIN_LIMIT)
#define LIST_INIT() do { \
      u32x4* lst = (u32x4*)(smem + L_SRC); \
      const u32 pm = tid < 2 * CW - 1024 ? 0x80000000u : 31u << 6; \
      const u32x4 P0 = {0x80000000u, 0x80000000u, 0x80000000u, 0x80000000u}, P1 = {31u << 6, 31u << 6, 31u << 6, 31u << 6}, PM = {pm, pm, pm, pm}; \
      lst[tid] = P0; lst[tid + 512] = P0; lst[tid + 1024] = PM; lst[tid + 1536] = P1; lst[tid + 2048] = P1; \
      if (tid < 4 * CW - 2560) lst[tid + 2560] = P1; \
      if (tid < 17) cntp[tid] = tid < 16 ? lsb + 4u * (u32)(tid >> 1) * CW + ((tid & 1) ? cav : cav - 4u) : 0u; } while (0)
#define LISTS_BAD() (__builtin_amdgcn_ballot_w64(relA > 4u * CW || relB > 4u * (CW - 127)) != 0)
  u32* hist = (u32*)(smem + L_HIST);
  if (tid < NBK) hist[tid] = ((u32)tid & 15u) << 4;
  asm volatile("gnn_pf:");
  BAR();
  u32 mb = ((u32)(32768 - H) << 16) + ((u32)i0 << 4);
  const u32 cpart4 = ((u32)b >> 7) << 2;
  u32 ra0, ra1, ra2, ha = ldsbase + (u32)L_HIST + ((u32)tid & ~3u), so = ((((u32)tid >> 2) * G1 + (u32)b) * 4u + ql) * 16u;
  {
    const u32 bk = (u32)tid >> 2, a0 = ql * 64u + bk * 16u;
    ra0 = ldsbase + bk * 256u + (a0 & 0xFFu); ra1 = ldsbase + bk * 256u + ((a0 + 16u) & 0xFFu); ra2 = ldsbase + bk * 256u + ((a0 + 32u) & 0xFFu);
  }
  asm volatile("" : "+v"(mb), "+v"(ra0), "+v"(ra1), "+v"(ra2), "+v"(ha), "+v"(so), "+v"(ntag), "+v"(xba));
#define K1_PRE(d, s, mbj, rk, bb, mw, z) do { \
    const u32 gw_ = (u32)(((u64)((d) + 1u) * magw) >> 32); u32 ws_; \
    asm("v_mul_u32_u24 %0, %1, %2" : "=v"(ws_) : "s"(N), "v"(gw_)); \
    const u32 sl_ = (d) - (ws_ >> 11), hb_ = gw_ >> 3; \
    rk = LDS_ADD(&hist[hb_], 65536u + 66u); \
    bb = (hb_ << 8) + ldsbase; \
    mw = (((s) << 16) + (mbj)) | ((sl_ >> 1) & 15u); \
    z = (gw_ & 7u) | ((sl_ << 3) & ~7u); } while (0)
#define K1_PUT(rk, bb, mw, z) do { u32x2 r_ = {mw, z}; asm volatile("ds_write_b64 %0, %1 offset:%2" :: "v"(((rk) & 0xF8u) | (bb)), "v"(r_), "n"(L_SEG) : "memory"); } while (0)
#define K1_ANY(rk, bb, mw, z) do { const u32 r2_ = (rk) >> 16; \
    if (r2_ < (u32)SCAP) K1_PUT(rk, bb, mw, z); \
    else if (r2_ < (u32)SCAP + 31u) { const u32x4 r_ = {mw, (((z) & 15u) << 3) | cpart4, (u32)tagx, (u32)(tagx >> 32)}; \
      __builtin_amdgcn_raw_buffer_store_b128(r_, rs, (int)(W_SEC + (((bb) - ldsbase + (u32)b) * 32u + (r2_ - (u32)SCAP)) * 16u), 0, K1AUX); } } while (0)
#define RANK4(dv, sv, mbase, nw) do { \
    asm volatile("s_waitcnt vmcnt(%2)" : "+v"(dv), "+v"(sv) : "n"(nw) : "memory"); \
    u32 rk[4], bb[4], mw[4], z[4]; \
    _Pragma("unroll") for (int j = 0; j < 4; ++j) K1_PRE(dv[j], sv[j], (mbase) + 16u * j, rk[j], bb[j], mw[j], z[j]); \
    asm volatile("" : "+v"(bb[0]), "+v"(bb[1]), "+v"(bb[2]), "+v"(bb[3]), "+v"(mw[0]), "+v"(mw[1]), "+v"(mw[2]), "+v"(mw[3]), "+v"(z[0]), "+v"(z[1]), "+v"(z[2]), "+v"(z[3])); \
    const u32 m01 = rk[0] > rk[1] ? rk[0] : rk[1], m23 = rk[2] > rk[3] ? rk[2] : rk[3]; \
    if (__builtin_expect(__builtin_amdgcn_ballot_w64((m01 > m23 ? m01 : m23) >= ((u32)SCAP << 16)) != 0, 0)) { \
      _Pragma("unroll 1") for (int j = 0; j < 4; ++j) { \
        const u32 rj = j == 0 ? rk[0] : j == 1 ? rk[1] : j == 2 ? rk[2] : rk[3]; \
        const u32 mj = j == 0 ? mw[0] : j == 1 ? mw[1] : j == 2 ? mw[2] : mw[3]; \
        const u32 zj = j == 0 ? z[0] : j == 1 ? z[1] : j == 2 ? z[2] : z[3]; \
        const u32 bj = j == 0 ? bb[0] : j == 1 ? bb[1] : j == 2 ? bb[2] : bb[3]; \
        K1_ANY(rj, bj, mj, zj); \
      } \
    } else { \
      _Pragma("unroll") for (int j = 0; j < 4; ++j) K1_PUT(rk[j], bb[j], mw[j], z[j]); \
    } } while (0)
  if (act1) RANK4(dv1, sv1, mb + 32768u, 3);
  if (act0) RANK4(dv0, sv0, mb, 1);
  if (__builtin_expect((cnt & 3) != 0, 0)) {
    if (tid < (cnt & 3)) {
      const int i = (cnt & ~3) + tid;
      const u32 d = (u32)dst_[ebase + i], sr = (u32)src_[ebase + i];
      u32 rk, bb, mw, z;
      K1_PRE(d, sr, ((u32)(32768 - H) << 16) + ((u32)i << 4), rk, bb, mw, z);
      K1_ANY(rk, bb, mw, z);
    }
  }
  asm volatile("s_waitcnt lgkmcnt(0)" ::: "memory");
  BAR();
  {
    u32x4 r[2][3]; u32 cn[2];
#pragma unroll
    for (int p = 0; p < 2; ++p)
      asm volatile("ds_read_b32 %0, %4 offset:%8\n\tds_read_b128 %1, %5 offset:%9\n\tds_read_b128 %2, %6 offset:%9\n\tds_read_b128 %3, %7 offset:%9"
                   : "=&v"(cn[p]), "=&v"(r[p][0]), "=&v"(r[p][1]), "=&v"(r[p][2]) : "v"(ha), "v"(ra0), "v"(ra1), "v"(ra2), "n"(512 * p), "n"(L_SEG + 32768 * p) : "memory");
#define BFI(m, a, c) (((m) & (a)) | (~(m) & (c)))
#pragma unroll
    for (int p = 0; p < 2; ++p) {
      asm volatile("s_waitcnt lgkmcnt(%4)" : "+v"(cn[p]), "+v"(r[p][0]), "+v"(r[p][1]), "+v"(r[p][2]) : "n"(4 - 4 * p) : "memory");
      const u32x4 r0 = r[p][0], r1 = r[p][1], r2 = r[p][2];
      const u32 c = cn[p] >> 16;
      const u32 qq = (c + 3u - ql) >> 2, kk = ((qq > 7u ? 7u : qq) << 12) | ntag;
      const u32x4 v0 = {r0[0], r0[2], r1[0], BFI(0xFFFu, BFI(0xFFu, BFI(15u, r0[1], r0[3] << 4), r1[1] << 8), kk)};
      const u32x4 v1 = {r1[2], r2[0], r2[2], BFI(0xFFFu, BFI(0xFFu, BFI(15u, r1[3], r2[1] << 4), r2[3] << 8), kk)};
      __builtin_amdgcn_raw_buffer_store_b128(v0, rs, (int)so, 128 * G1 * 64 * p, K1AUX);
      if (c > 12u) {
        __builtin_amdgcn_raw_buffer_store_b128(v1, rs, (int)so, (int)W_P1 + 128 * G1 * 64 * p, K1AUX);
        if (__builtin_expect(c > (u32)SCAP && ql == 0u, 0)) {
          const u32x4 hd = {c, 0u, (u32)tagx, (u32)(tagx >> 32)};
          __builtin_amdgcn_raw_buffer_store_b128(hd, rs, (int)(8u * so + 31u * 16u), (int)W_SEC + 128 * G1 * 512 * p, K1AUX);
        }
      }
    }
  }
  SB();
  W_LOAD();
  XB_LOAD();
  SB();
  LIST_INIT();
  asm volatile("" : "+{v[230:233]}"(lp), "+{v[234:237]}"(aov), "+{v[238:241]}"(mi1), "+{v[242:245]}"(mi2), "+v"(ro0), "+v"(ro1), "+v"(cb0), "+v"(cb1));
  BAR();
  {
    W_WAIT(4);
    const float sc = wid < 4 ? -1.4426950408889634f : 1.f;
    u32x4 f0, f1, g0, g1;
#pragma unroll
    for (int q = 0; q < 4; ++q) {
      f0[q] = cvtpk(U2F(wA[2 * q] << 16) * sc, U2F(wA[2 * q + 1] << 16) * sc);
      f1[q] = cvtpk(U2F(wA[2 * q] & 0xFFFF0000u) * sc, U2F(wA[2 * q + 1] & 0xFFFF0000u) * sc);
      g0[q] = __builtin_amdgcn_perm(wB[2 * q + 1], wB[2 * q], 0x05040100u);
      g1[q] = __builtin_amdgcn_perm(wB[2 * q + 1], wB[2 * q], 0x07060302u);
    }
    char* ia = smem + L_IMG + 8192 * (wid >> 2) + 1024 * (wid & 3) + l16;
    *(u32x4*)ia = f0; *(u32x4*)(ia + 4096) = f1;
    const u32 c2 = cvtpk(U2F(wC << 16) * -1.4426950408889634f, U2F(wC & 0xFFFF0000u) * -1.4426950408889634f);
    *(u16*)(smem + L_IMG + 16 * I_WC + l16 + 2 * wid) = (u16)c2; *(u16*)(smem + L_IMG + 16 * I_WC + 1024 + l16 + 2 * wid) = (u16)(c2 >> 16);
    if (wid < 4) {
      *(u32x4*)(smem + L_WV + 1024 * wid + l16) = g0; *(u32x4*)(smem + L_WV + 4096 + 1024 * wid + l16) = g1;
    } else {
      const int wo = (int)(W_WU + 8192u * (u32)b + (u32)(128 * r32 + 32 * (wid & 3) + 16 * h));
      __builtin_amdgcn_raw_buffer_store_b128(g0, rs, wo, 0, WUAUX);
      __builtin_amdgcn_raw_buffer_store_b128(g1, rs, wo + 4096, 0, WUAUX);
      const bool t2 = wid >= 6;
      const int d = 16 * (wid & 1) + (lane >> 2), sh = lane & 3;
      const int g = t2 ? d >> 3 : ((d >> 3) & 2) | ((d >> 2) & 1), j = t2 ? d & 7 : ((d >> 1) & 4) | (d & 3);
      const u32 one = g == sh ? 0x3F80u << (16 * (j & 1)) : 0u;
      u32x4 o;
#pragma unroll
      for (int q = 0; q < 4; ++q) o[q] = (j >> 1) == q ? one : 0u;
      *(u32x4*)(smem + L_T1 + 1024 * (wid & 3) + l16) = o;
    }
  }
  SB();
  while (__builtin_amdgcn_s_memrealtime() - t_start < DELAY) __builtin_amdgcn_s_sleep(1);
  REC_LOAD(~0ull, ~0ull, ~0ull, ~0ull);
  {
    u64 pend[4] = {~0ull, ~0ull, ~0ull, ~0ull}, mb0 = 0, mb1 = 0;
#pragma unroll 1
    for (;;) {
      u64 nv[4]; int qn[4];
      REC_TAKE();
#pragma unroll
      for (int k = 0; k < 4; ++k) {
        u32 t = rec[k][3] ^ ntag;
        t |= (u32)__builtin_amdgcn_mov_dpp((int)t, 0xB1, 0xF, 0xF, true);
        t |= (u32)__builtin_amdgcn_mov_dpp((int)t, 0x4E, 0xF, 0xF, true);
        nv[k] = __builtin_amdgcn_ballot_w64(t < 65536u) & pend[k];
        qn[k] = (int)__builtin_amdgcn_ubfe(rec[k][3], 12, 3);
        pend[k] &= ~nv[k];
      }
#pragma unroll
      for (int i = 0; i < 2; ++i) {
        const u64 m = __builtin_amdgcn_ballot_w64(qn[i] <= 3) & nv[i] & 0x1111111111111111ull;
        pend[2 + i] &= ~((m << 4) - m);
      }
      const bool more = ((pend[0] | pend[1]) | (pend[2] | pend[3])) != 0;
      REC_LOAD(pend[0], pend[1], pend[2], pend[3]);
      PREFETCH();
      mb0 |= __builtin_amdgcn_ballot_w64(qn[0] == 7) & nv[0]; mb1 |= __builtin_amdgcn_ballot_w64(qn[1] == 7) & nv[1];
      SLOT_A(0, 1, 0) SLOT_A(1, 1, 1) SLOT_A(2, 1, 2) SLOT_A(3, 3, 0) SLOT_A(4, 3, 1) SLOT_A(5, 3, 2)
      SLOT_A(6, 2, 2) SLOT_A(7, 2, 1) SLOT_A(8, 2, 0) SLOT_A(9, 0, 2) SLOT_A(10, 0, 1) SLOT_A(11, 0, 0)
      asm volatile("s_waitcnt lgkmcnt(6)" : "+v"(pos0), "+v"(pos1), "+v"(pos2), "+v"(pos3), "+v"(pos4), "+v"(pos5) :: "memory");
      SLOT_B(0); SLOT_B(1); SLOT_B(2); SLOT_B(3); SLOT_B(4); SLOT_B(5);
      asm volatile("s_waitcnt lgkmcnt(6)" : "+v"(pos6), "+v"(pos7), "+v"(pos8), "+v"(pos9), "+v"(pos10), "+v"(pos11) :: "memory");
      SLOT_B(6); SLOT_B(7); SLOT_B(8); SLOT_B(9); SLOT_B(10); SLOT_B(11);
      if (!more) break;
      if (__builtin_expect(TIMED_OUT(), 0)) { REC_TAKE(); GIVE_UP(); mb0 = mb1 = 0; break; }
    }
    mb0 &= 0x1111111111111111ull; mb1 &= 0x1111111111111111ull;
#pragma unroll 1
    while (__builtin_expect((mb0 | mb1) != 0, 0)) {
      const int i_ = mb0 ? 0 : 1;
      const int l_ = __builtin_ctzll(mb0 ? mb0 : mb1);
      const u32 sc = (u32)__builtin_amdgcn_readlane((int)(i_ ? sg1 : sg0), l_);
      u32x4 rr;
      asm volatile("global_load_dwordx4 %0, %1, %2 " RECP "\n\ts_waitcnt vmcnt(0)" : "=&v"(rr) : "v"(W_SEC + (((u32)b * G1 + sc) * 32u + (u32)r32) * 16u), "s"(prim0) : "memory");
      const u32 ok = (u32)__builtin_amdgcn_ballot_w64(rr[2] == (u32)tagx && rr[3] == (u32)(tagx >> 32));
      const u32 cs = (u32)__builtin_amdgcn_readlane((int)rr[0], 31);
      if ((int)ok < 0 && cs > (u32)SCAP + 31u) { GIVE_UP(); break; }
      const u32 need = 0x80000000u | ((1u << ((cs - (u32)SCAP) & 31u)) - 1u);
      if ((ok & need) != need) {
        if (TIMED_OUT()) { GIVE_UP(); break; }
        continue;
      }
      if (mb0) mb0 &= mb0 - 1; else mb1 &= mb1 - 1;
      if ((u32)lane + (u32)SCAP < cs) {
        const u32 p_ = LDS_ADD(&cntp[(rr[1] >> 2) & 15u], (rr[1] & 4u) ? 4u : ~3u) - lsb;
        if (p_ < 4u * 8u * CW) {
          *(u32*)(smem + L_SRC + p_) = rr[0];
          *(u32*)(smem + L_EID + p_) = ((rr[0] & 0xFFFFu) << 7) + ((sc * (u32)CH) << 11) + rr[1];
        }
      }
    }
    asm volatile("s_waitcnt lgkmcnt(0)" : "+v"(basei), "+s"(xs1), "+s"(wu1) :: "memory");
    BAR();
    const u32x2 ci = *(const u32x2*)(smem + L_CNT + 8 * q8), cj = *(const u32x2*)(smem + L_CNT + 8 * p8);
    relA = ci[0] + 4u - basei;
    relB = ci[1] - basei;
    keyi = ((ci[1] - ci[0]) << 1) + (u32)q8; keyj = ((cj[1] - cj[0]) << 1) + (u32)p8;
  }
  if (__builtin_expect(LISTS_BAD(), 0)) {
    bool slow = __builtin_amdgcn_readfirstlane((int)cntp[16]) != 0;
    if (!slow) {
      cav = (cav - (u32)__builtin_amdgcn_ds_bpermute(32 * ((lane >> 1) & 7), (int)relA) + 15u) & ~15u;
      BAR();
      {
        typedef const u16* cp16;
        const __attribute__((address_space(4))) cp16* ka = (const __attribute__((address_space(4))) cp16*)__builtin_amdgcn_kernarg_segment_ptr();
        asm volatile("" : "+s"(ka));
        const u16* wa_ = ka[8]; const u16* wb_ = ka[9]; const u16* wc_ = ka[10]; const u16* wu_ = ka[11]; const u16* wv_ = ka[12];
        int q8r = q8;
        asm volatile("" : "+v"(q8r));
        u32x4 xba;
#pragma unroll
        for (int i = 0; i < 4; ++i) {
          const int row = wn0 - n0 + 8 * i + q8r;
          int node = n0 + row; node = node < N ? node : N - 1;
          xba[i] = ((u32)node << 7) + 16u * (u32)(p8 ^ ((row >> 1) & 7));
        }
        W_LOAD();
        XB_LOAD();
        LIST_INIT();
  {
    W_WAIT(4);
    const float sc = wid < 4 ? -1.4426950408889634f : 1.f;
    u32x4 f0, f1, g0, g1;
#pragma unroll
    for (int q = 0; q < 4; ++q) {
      f0[q] = cvtpk(U2F(wA[2 * q] << 16) * sc, U2F(wA[2 * q + 1] << 16) * sc);
      f1[q] = cvtpk(U2F(wA[2 * q] & 0xFFFF0000u) * sc, U2F(wA[2 * q + 1] & 0xFFFF0000u) * sc);
      g0[q] = __builtin_amdgcn_perm(wB[2 * q + 1], wB[2 * q], 0x05040100u);
      g1[q] = __builtin_amdgcn_perm(wB[2 * q + 1], wB[2 * q], 0x07060302u);
    }
    char* ia = smem + L_IMG + 8192 * (wid >> 2) + 1024 * (wid & 3) + l16;
    *(u32x4*)ia = f0; *(u32x4*)(ia + 4096) = f1;
    const u32 c2 = cvtpk(U2F(wC << 16) * -1.4426950408889634f, U2F(wC & 0xFFFF0000u) * -1.4426950408889634f);
    *(u16*)(smem + L_IMG + 16 * I_WC + l16 + 2 * wid) = (u16)c2; *(u16*)(smem + L_IMG + 16 * I_WC + 1024 + l16 + 2 * wid) = (u16)(c2 >> 16);
    if (wid < 4) {
      *(u32x4*)(smem + L_WV + 1024 * wid + l16) = g0; *(u32x4*)(smem + L_WV + 4096 + 1024 * wid + l16) = g1;
    } else {
      const int wo = (int)(W_WU + 8192u * (u32)b + (u32)(128 * r32 + 32 * (wid & 3) + 16 * h));
      __builtin_amdgcn_raw_buffer_store_b128(g0, rs, wo, 0, WUAUX);
      __builtin_amdgcn_raw_buffer_store_b128(g1, rs, wo + 4096, 0, WUAUX);
      const bool t2 = wid >= 6;
      const int d = 16 * (wid & 1) + (lane >> 2), sh = lane & 3;
      const int g = t2 ? d >> 3 : ((d >> 3) & 2) | ((d >> 2) & 1), j = t2 ? d & 7 : ((d >> 1) & 4) | (d & 3);
      const u32 one = g == sh ? 0x3F80u << (16 * (j & 1)) : 0u;
      u32x4 o;
#pragma unroll
      for (int q = 0; q < 4; ++q) o[q] = (j >> 1) == q ? one : 0u;
      *(u32x4*)(smem + L_T1 + 1024 * (wid & 3) + l16) = o;
    }
  }
      }
      REC_LOAD(~0ull, ~0ull, ~0ull, ~0ull);
      BAR();
    u64 pend[4] = {~0ull, ~0ull, ~0ull, ~0ull}, mb0 = 0, mb1 = 0;
#pragma unroll 1
    for (;;) {
      u64 nv[4]; int qn[4];
      REC_TAKE();
#pragma unroll
      for (int k = 0; k < 4; ++k) {
        u32 t = rec[k][3] ^ ntag;
        t |= (u32)__builtin_amdgcn_mov_dpp((int)t, 0xB1, 0xF, 0xF, true);
        t |= (u32)__builtin_amdgcn_mov_dpp((int)t, 0x4E, 0xF, 0xF, true);
        nv[k] = __builtin_amdgcn_ballot_w64(t < 65536u) & pend[k];
        qn[k] = (int)__builtin_amdgcn_ubfe(rec[k][3], 12, 3);
        pend[k] &= ~nv[k];
      }
#pragma unroll
      for (int i = 0; i < 2; ++i) {
        const u64 m = __builtin_amdgcn_ballot_w64(qn[i] <= 3) & nv[i] & 0x1111111111111111ull;
        pend[2 + i] &= ~((m << 4) - m);
      }
      const bool more = ((pend[0] | pend[1]) | (pend[2] | pend[3])) != 0;
      REC_LOAD(pend[0], pend[1], pend[2], pend[3]);
      PREFETCH();
      mb0 |= __builtin_amdgcn_ballot_w64(qn[0] == 7) & nv[0]; mb1 |= __builtin_amdgcn_ballot_w64(qn[1] == 7) & nv[1];
      SLOT_A(0, 1, 0) SLOT_A(1, 1, 1) SLOT_A(2, 1, 2) SLOT_A(3, 3, 0) SLOT_A(4, 3, 1) SLOT_A(5, 3, 2)
      SLOT_A(6, 2, 2) SLOT_A(7, 2, 1) SLOT_A(8, 2, 0) SLOT_A(9, 0, 2) SLOT_A(10, 0, 1) SLOT_A(11, 0, 0)
      asm volatile("s_waitcnt lgkmcnt(6)" : "+v"(pos0), "+v"(pos1), "+v"(pos2), "+v"(pos3), "+v"(pos4), "+v"(pos5) :: "memory");
      SLOT_B(0); SLOT_B(1); SLOT_B(2); SLOT_B(3); SLOT_B(4); SLOT_B(5);
      asm volatile("s_waitcnt lgkmcnt(6)" : "+v"(pos6), "+v"(pos7), "+v"(pos8), "+v"(pos9), "+v"(pos10), "+v"(pos11) :: "memory");
      SLOT_B(6); SLOT_B(7); SLOT_B(8); SLOT_B(9); SLOT_B(10); SLOT_B(11);
      if (!more) break;
      if (__builtin_expect(TIMED_OUT(), 0)) { REC_TAKE(); GIVE_UP(); mb0 = mb1 = 0; break; }
    }
    mb0 &= 0x1111111111111111ull; mb1 &= 0x1111111111111111ull;
#pragma unroll 1
    while (__builtin_expect((mb0 | mb1) != 0, 0)) {
      const int i_ = mb0 ? 0 : 1;
      const int l_ = __builtin_ctzll(mb0 ? mb0 : mb1);
      const u32 sc = (u32)__builtin_amdgcn_readlane((int)(i_ ? sg1 : sg0), l_);
      u32x4 rr;
      asm volatile("global_load_dwordx4 %0, %1, %2 " RECP "\n\ts_waitcnt vmcnt(0)" : "=&v"(rr) : "v"(W_SEC + (((u32)b * G1 + sc) * 32u + (u32)r32) * 16u), "s"(prim0) : "memory");
      const u32 ok = (u32)__builtin_amdgcn_ballot_w64(rr[2] == (u32)tagx && rr[3] == (u32)(tagx >> 32));
      const u32 cs = (u32)__builtin_amdgcn_readlane((int)rr[0], 31);
      if ((int)ok < 0 && cs > (u32)SCAP + 31u) { GIVE_UP(); break; }
      const u32 need = 0x80000000u | ((1u << ((cs - (u32)SCAP) & 31u)) - 1u);
      if ((ok & need) != need) {
        if (TIMED_OUT()) { GIVE_UP(); break; }
        continue;
      }
      if (mb0) mb0 &= mb0 - 1; else mb1 &= mb1 - 1;
      if ((u32)lane + (u32)SCAP < cs) {
        const u32 p_ = LDS_ADD(&cntp[(rr[1] >> 2) & 15u], (rr[1] & 4u) ? 4u : ~3u) - lsb;
        if (p_ < 4u * 8u * CW) {
          *(u32*)(smem + L_SRC + p_) = rr[0];
          *(u32*)(smem + L_EID + p_) = ((rr[0] & 0xFFFFu) << 7) + ((sc * (u32)CH) << 11) + rr[1];
        }
      }
    }
    asm volatile("s_waitcnt lgkmcnt(0)" : "+v"(basei), "+s"(xs1), "+s"(wu1) :: "memory");
    BAR();
    const u32x2 ci = *(const u32x2*)(smem + L_CNT + 8 * q8), cj = *(const u32x2*)(smem + L_CNT + 8 * p8);
    relA = ci[0] + 4u - basei;
    relB = ci[1] - basei;
    keyi = ((ci[1] - ci[0]) << 1) + (u32)q8; keyj = ((cj[1] - cj[0]) << 1) + (u32)p8;
      slow = LISTS_BAD();
    }
    if (slow) {
      typedef const u16* cp16;
      const __attribute__((address_space(4))) cp16* ka = (const __attribute__((address_space(4))) cp16*)__builtin_amdgcn_kernarg_segment_ptr();
      asm volatile("" : "+s"(ka));
      const int* src = (const int*)ka[6]; const int* dst = (const int*)ka[7];
      const u16* wa = ka[8]; const u16* wb = ka[9]; const u16* wc = ka[10]; const u16* wu = ka[11]; const u16* wv = ka[12];
      const int nn = WSTART(8 * b + 8) - n0;
      float* acc = (float*)smem;
      asm volatile("s_waitcnt vmcnt(0)" ::: "memory");
      BAR();
      for (int i = tid; i < nn * 128; i += 512) acc[i] = 0.f;
      BAR();
      for (int e0 = wid * 64; e0 < E; e0 += 512) {
        const int dd = e0 + lane < E ? dst[e0 + lane] - n0 : -1;
        u64 m = __builtin_amdgcn_ballot_w64(dd >= 0 && dd < nn);
        while (m) {
          const int l = __builtin_ctzll(m); m &= m - 1;
          const int dl = __builtin_amdgcn_readlane(dd, l), e = e0 + l;
          const u16* xs = x + (size_t)src[e] * 64;
          const u16* xd = x + (size_t)(n0 + dl) * 64;
          float g = 0.f, hv = 0.f;
          for (int kk = 0; kk < 64; ++kk) {
            const float a = bf2f(xs[kk]);
            g += bf2f(xd[kk]) * bf2f(wa[kk * 64 + lane]) + a * bf2f(wb[kk * 64 + lane]);
            hv += a * bf2f(wv[kk * 64 + lane]);
          }
          for (int kk = 0; kk < 16; ++kk) g += bf2f(ea[(size_t)e * 16 + kk]) * bf2f(wc[kk * 64 + lane]);
          const float eta = 1.f / (1.f + __builtin_amdgcn_exp2f(g * -1.4426950408889634f));
          LDS_ADD(&acc[dl * 128 + lane], eta);
          LDS_ADD(&acc[dl * 128 + 64 + lane], eta * hv);
        }
      }
      BAR();
      for (int i = tid; i < nn * 32; i += 512) {
        const int n = i >> 5, c2 = i & 31;
        const u16* xr = x + (size_t)(n0 + n) * 64;
        float o[2];
        for (int t = 0; t < 2; ++t) {
          const int f = 2 * c2 + t;
          float z = 0.f;
          for (int kk = 0; kk < 64; ++kk) z += bf2f(xr[kk]) * bf2f(wu[kk * 64 + f]);
          z += acc[n * 128 + 64 + f] / (acc[n * 128 + f] + 1e-6f);
          o[t] = bf2f(xr[f]) + (z > 0.f ? z : 0.f);
        }
        out[(size_t)(n0 + n) * 32 + c2] = cvtpk(o[0], o[1]);
      }
      return;
    }
  }
  int win;
  {
    const u64 m = __builtin_amdgcn_ballot_w64(keyj > keyi);
    const u32 rank = (u32)__builtin_popcount((u32)(m >> (8 * p8)) & 0xFFu);
    win = __builtin_ctz((u32)__builtin_amdgcn_ballot_w64(rank == (u32)(wid < 4 ? wid : 11 - wid)));
  }
  const u32 s0a = (u32)__builtin_amdgcn_readlane((int)relA, 8 * win) & ~15u;
  const int ntile = (int)(((u32)__builtin_amdgcn_readlane((int)relB, 8 * win) - s0a + 127u) >> 7);
  const u32 lb0 = lsb + (u32)(4 * CW * win) + s0a;
  const int wabs = WSTART(8 * b + win), wbase = wabs - n0;
  f32x16 yd[2], yn[2];
  u32x16 axo;
    asm volatile(
      "s_nop 1\n"
      "v_add_u32 v230, %[lb0], v230\n"
      "v_add_u32 v231, %[lb0], v231\n"
      "s_lshl_b32 s87, %[nt], 7\n"
      "v_lshrrev_b32 v136, 2, %[l16]\n"
      "s_add_u32 s87, s87, %[lb0]\n"
      "v_mov_b32 v137, 0x7c0\n"
      "v_add_u32 v136, s87, v136\n"
      "ds_write2st64_b32 v136, v245, v137 offset1:83\n"
      "ds_read_b128 v[220:223], v230 offset:0\n"
      "ds_read_b32 v228, v231 offset:0\n"
      "ds_read_b128 v[224:227], v230 offset:128\n"
      "ds_read_b32 v229, v231 offset:128\n"
      "s_movk_i32 s85, 0x80\n"
      "s_cmp_gt_i32 %[nt], 0\n"
      "s_cselect_b64 s[88:89], %[xs1], %[wu1]\n"
      "s_add_u32 m0, %[stg], 4096\n"
      "v_bfe_u32 v145, %[l16], 4, 5\n"
      "v_lshrrev_b32 v146, 9, %[l16]\n"
      "v_add_u32 v145, %[wbs], v145\n"
      "v_bfe_u32 v141, v145, 1, 3\n"
      "v_lshl_add_u32 v145, v145, 7, %[xbb]\n"
      "v_xor_b32 v146, v146, v141\n"
      "v_add_u32 v144, %[img], %[l16]\n"
      "v_lshl_add_u32 v140, v146, 4, v145\n"
      "v_xor_b32 v141, 32, v140\n"
      "v_xor_b32 v142, 64, v140\n"
      "v_xor_b32 v143, 0x60, v140\n"
      "s_waitcnt lgkmcnt(2)\n"
      "v_lshrrev_b32 v228, 11, v228\n"
      "v_lshl_add_u32 v228, v228, 5, v244\n"
      "v_mad_u32_u16 v220, v220, s85, v242 op_sel:[1,0,0,0]\n"
      "global_load_lds_dwordx4 v228, %[ea] sc1\n"
      "s_mov_b32 m0, %[stg]\n"
      "v_mad_u32_u16 v221, v221, s85, v243 op_sel:[1,0,0,0]\n"
      "global_load_lds_dwordx4 v220, s[88:89]\n"
      "global_load_lds_dwordx4 v221, s[88:89] offset:1024\n"
      "s_add_u32 m0, %[stg], 2048\n"
      "v_mad_u32_u16 v222, v222, s85, v242 op_sel:[1,0,0,0]\n"
      "v_mad_u32_u16 v223, v223, s85, v243 op_sel:[1,0,0,0]\n"
      "global_load_lds_dwordx4 v222, s[88:89]\n"
      "global_load_lds_dwordx4 v223, s[88:89] offset:1024\n"
      "ds_read_b128 v[188:191], v140 offset:0\n"
      "ds_read_b128 v[156:159], v144 offset:8192\n"
      "ds_read_b128 v[172:175], v144 offset:12288\n"
      "ds_read_b128 v[192:195], v141 offset:0\n"
      "ds_read_b128 v[160:163], v144 offset:9216\n"
      "ds_read_b128 v[176:179], v144 offset:13312\n"
      "ds_read_b128 v[196:199], v142 offset:0\n"
      "ds_read_b128 v[164:167], v144 offset:10240\n"
      "ds_read_b128 v[180:183], v144 offset:14336\n"
      "ds_read_b128 v[200:203], v143 offset:0\n"
      "ds_read_b128 v[168:171], v144 offset:11264\n"
      "ds_read_b128 v[184:187], v144 offset:15360\n"
      "s_cmp_gt_i32 %[nt], 1\n"
      "s_cselect_b64 s[88:89], %[xs1], %[wu1]\n"
      "s_add_u32 m0, %[stg], 45056\n"
      "s_waitcnt lgkmcnt(12)\n"
      "v_lshrrev_b32 v229, 11, v229\n"
      "v_lshl_add_u32 v229, v229, 5, v244\n"
      "v_mad_u32_u16 v224, v224, s85, v242 op_sel:[1,0,0,0]\n"
      "v_mad_u32_u16 v225, v225, s85, v243 op_sel:[1,0,0,0]\n"
      "v_mad_u32_u16 v226, v226, s85, v242 op_sel:[1,0,0,0]\n"
      "v_mad_u32_u16 v227, v227, s85, v243 op_sel:[1,0,0,0]\n"
      "v_add_u32 v232, %[lb0], v232\n"
      "v_add_u32 v233, %[lb0], v233\n"
      "ds_read_b128 v[64:67], v144 offset:0\n"
      "ds_read_b128 v[68:71], v144 offset:1024\n"
      "ds_read_b128 v[72:75], v144 offset:2048\n"
      "s_waitcnt lgkmcnt(12)\n"
      "v_mfma_f32_32x32x16_bf16 v[0:15], v[188:191], v[156:159], 0\n"
      "v_mfma_f32_32x32x16_bf16 v[16:31], v[188:191], v[172:175], 0\n"
      "ds_read_b128 v[76:79], v144 offset:3072\n"
      "ds_read_b128 v[80:83], v144 offset:4096\n"
      "ds_read_b128 v[84:87], v144 offset:5120\n"
      "s_waitcnt lgkmcnt(12)\n"
      "v_mfma_f32_32x32x16_bf16 v[0:15], v[192:195], v[160:163], v[0:15]\n"
      "v_mfma_f32_32x32x16_bf16 v[16:31], v[192:195], v[176:179], v[16:31]\n"
      "ds_read_b128 v[88:91], v144 offset:6144\n"
      "ds_read_b128 v[92:95], v144 offset:7168\n"
      "ds_read_b128 v[96:99], v144 offset:16384\n"
      "s_waitcnt lgkmcnt(12)\n"
      "v_mfma_f32_32x32x16_bf16 v[0:15], v[196:199], v[164:167], v[0:15]\n"
      "v_mfma_f32_32x32x16_bf16 v[16:31], v[196:199], v[180:183], v[16:31]\n"
      "ds_read_b128 v[100:103], v144 offset:17408\n"
      "s_waitcnt lgkmcnt(10)\n"
      "v_mfma_f32_32x32x16_bf16 v[0:15], v[200:203], v[168:171], v[0:15]\n"
      "v_mfma_f32_32x32x16_bf16 v[16:31], v[200:203], v[184:187], v[16:31]\n"
      "s_waitcnt lgkmcnt(0)\n"
      "s_barrier\n"
      "global_load_lds_dwordx4 v229, %[ea] sc1\n"
      "s_add_u32 m0, %[stg], 40960\n"
      "s_nop 0\n"
      "global_load_lds_dwordx4 v224, s[88:89]\n"
      "global_load_lds_dwordx4 v225, s[88:89] offset:1024\n"
      "s_add_u32 m0, %[stg], 43008\n"
      "s_nop 0\n"
      "global_load_lds_dwordx4 v226, s[88:89]\n"
      "global_load_lds_dwordx4 v227, s[88:89] offset:1024\n"
      "s_movk_i32 s84, 0x7c0\n"
      "s_mov_b32 s86, 0xbfb8aa3b\n"
      "ds_read_b32 v229, v232 offset:0\n"
      "ds_read_b128 v[224:227], v233 offset:0\n"
      "ds_read_b128 v[204:207], v239 offset:0\n"
      "ds_read_b128 v[208:211], v239 offset:1024\n"
      "ds_read_b128 v[212:215], v239 offset:2048\n"
      "ds_read_b128 v[216:219], v239 offset:3072\n"
      "ds_read_b128 v[220:223], v230 offset:256\n"
      "ds_read_b32 v228, v231 offset:256\n"
      "v_mul_f32 v0, s86, v0\n"
      "v_mul_f32 v1, s86, v1\n"
      "v_cvt_pk_bf16_f32 v104, v0, v1\n"
      "v_mul_f32 v2, s86, v2\n"
      "v_mul_f32 v3, s86, v3\n"
      "v_cvt_pk_bf16_f32 v105, v2, v3\n"
      "v_mul_f32 v4, s86, v4\n"
      "v_mul_f32 v5, s86, v5\n"
      "v_cvt_pk_bf16_f32 v106, v4, v5\n"
      "v_mul_f32 v6, s86, v6\n"
      "v_mul_f32 v7, s86, v7\n"
      "v_cvt_pk_bf16_f32 v107, v6, v7\n"
      "v_mul_f32 v8, s86, v8\n"
      "v_mul_f32 v9, s86, v9\n"
      "v_cvt_pk_bf16_f32 v108, v8, v9\n"
      "v_mul_f32 v10, s86, v10\n"
      "v_mul_f32 v11, s86, v11\n"
      "v_cvt_pk_bf16_f32 v109, v10, v11\n"
      "v_mul_f32 v12, s86, v12\n"
      "v_mul_f32 v13, s86, v13\n"
      "v_cvt_pk_bf16_f32 v110, v12, v13\n"
      "v_mul_f32 v14, s86, v14\n"
      "v_mul_f32 v15, s86, v15\n"
      "v_cvt_pk_bf16_f32 v111, v14, v15\n"
      "v_mul_f32 v16, s86, v16\n"
      "v_mul_f32 v17, s86, v17\n"
      "v_cvt_pk_bf16_f32 v112, v16, v17\n"
      "v_mul_f32 v18, s86, v18\n"
      "v_mul_f32 v19, s86, v19\n"
      "v_cvt_pk_bf16_f32 v113, v18, v19\n"
      "v_mul_f32 v20, s86, v20\n"
      "v_mul_f32 v21, s86, v21\n"
      "v_cvt_pk_bf16_f32 v114, v20, v21\n"
      "v_mul_f32 v22, s86, v22\n"
      "v_mul_f32 v23, s86, v23\n"
      "v_cvt_pk_bf16_f32 v115, v22, v23\n"
      "v_mul_f32 v24, s86, v24\n"
      "v_mul_f32 v25, s86, v25\n"
      "v_cvt_pk_bf16_f32 v116, v24, v25\n"
      "v_mul_f32 v26, s86, v26\n"
      "v_mul_f32 v27, s86, v27\n"
      "v_cvt_pk_bf16_f32 v117, v26, v27\n"
      "v_mul_f32 v28, s86, v28\n"
      "v_mul_f32 v29, s86, v29\n"
      "v_cvt_pk_bf16_f32 v118, v28, v29\n"
      "v_mul_f32 v30, s86, v30\n"
      "v_mul_f32 v31, s86, v31\n"
      "v_cvt_pk_bf16_f32 v119, v30, v31\n"
      "s_movk_i32 s86, 0x100\n"
      "v_mov_b32 v152, 0\n"
      "v_mov_b32 v153, 0\n"
      "v_mov_b32 v154, 0\n"
      "v_mov_b32 v155, 0\n"
      "v_mov_b32 v180, 0\n"
      "v_mov_b32 v181, 0\n"
      "v_mov_b32 v182, 0\n"
      "v_mov_b32 v183, 0\n"
      "v_mov_b32 v196, 0\n"
      "v_mov_b32 v197, 0\n"
      "v_mov_b32 v198, 0\n"
      "v_mov_b32 v199, 0\n"
      "v_mov_b64 v[0:1], 0\n"
      "v_mov_b64 v[2:3], 0\n"
      "v_mov_b64 v[4:5], 0\n"
      "v_mov_b64 v[6:7], 0\n"
      "v_mov_b64 v[8:9], 0\n"
      "v_mov_b64 v[10:11], 0\n"
      "v_mov_b64 v[12:13], 0\n"
      "v_mov_b64 v[14:15], 0\n"
      "v_mov_b64 v[16:17], 0\n"
      "v_mov_b64 v[18:19], 0\n"
      "v_mov_b64 v[20:21], 0\n"
      "v_mov_b64 v[22:23], 0\n"
      "v_mov_b64 v[24:25], 0\n"
      "v_mov_b64 v[26:27], 0\n"
      "v_mov_b64 v[28:29], 0\n"
      "v_mov_b64 v[30:31], 0\n"
      "v_mov_b64 v[32:33], 0\n"
      "v_mov_b64 v[34:35], 0\n"
      "v_mov_b64 v[36:37], 0\n"
      "v_mov_b64 v[38:39], 0\n"
      "v_mov_b64 v[40:41], 0\n"
      "v_mov_b64 v[42:43], 0\n"
      "v_mov_b64 v[44:45], 0\n"
      "v_mov_b64 v[46:47], 0\n"
      "v_mov_b64 v[48:49], 0\n"
      "v_mov_b64 v[50:51], 0\n"
      "v_mov_b64 v[52:53], 0\n"
      "v_mov_b64 v[54:55], 0\n"
      "v_mov_b64 v[56:57], 0\n"
      "v_mov_b64 v[58:59], 0\n"
      "v_mov_b64 v[60:61], 0\n"
      "v_mov_b64 v[62:63], 0\n"
      "s_cmp_gt_i32 %[nt], 2\n"
      "s_cselect_b64 s[88:89], %[xs1], %[wu1]\n"
      "s_sub_u32 s87, %[nt], 1\n"
      "s_waitcnt vmcnt(5) lgkmcnt(6)\n"
      "v_and_or_b32 v229, v229, s84, v240\n"
      "ds_read_b128 v[136:139], v238 offset:0\n"
      "ds_read_b128 v[120:123], v234 offset:0\n"
      "ds_read_b128 v[124:127], v235 offset:0\n"
      "ds_read_b128 v[128:131], v236 offset:0\n"
      "ds_read_b128 v[132:135], v237 offset:0\n"
      "ds_read_b128 v[140:143], v229 offset:0\n"
      "ds_read_b128 v[144:147], v229 offset:32\n"
      "v_and_or_b32 v224, v224, s84, v241\n"
      "v_and_or_b32 v225, v225, s84, v241\n"
      "v_and_or_b32 v226, v226, s84, v241\n"
      "v_and_or_b32 v227, v227, s84, v241\n"
      "s_waitcnt lgkmcnt(7)\n"
      "v_lshrrev_b32 v228, 11, v228\n"
      "v_lshl_add_u32 v228, v228, 5, v244\n"
      "v_mad_u32_u16 v220, v220, s85, v242 op_sel:[1,0,0,0]\n"
      "v_mad_u32_u16 v221, v221, s85, v243 op_sel:[1,0,0,0]\n"
      "v_mad_u32_u16 v222, v222, s85, v242 op_sel:[1,0,0,0]\n"
      "v_mad_u32_u16 v223, v223, s85, v243 op_sel:[1,0,0,0]\n"
      "s_add_u32 m0, %[stg], 4096\n"
      "s_waitcnt lgkmcnt(0)\n"
      "global_load_lds_dwordx4 v228, %[ea] sc1\n"
      "s_mov_b32 m0, %[stg]\n"
      "s_nop 0\n"
      "global_load_lds_dwordx4 v220, s[88:89]\n"
      "global_load_lds_dwordx4 v221, s[88:89] offset:1024\n"
      "s_add_u32 m0, %[stg], 2048\n"
      "s_nop 0\n"
      "global_load_lds_dwordx4 v222, s[88:89]\n"
      "global_load_lds_dwordx4 v223, s[88:89] offset:1024\n"
      "s_cmp_eq_u32 %[nt], 0\n"
      "s_cbranch_scc1 3f\n"
      "v_mfma_f32_32x32x16_bf16 v[156:171], v[140:143], v[104:107], 0\n"
      "v_mfma_f32_32x32x16_bf16 v[156:171], v[144:147], v[108:111], v[156:171]\n"
      "v_mfma_f32_32x32x16_bf16 v[156:171], v[136:139], v[96:99], v[156:171]\n"
      "v_mfma_f32_32x32x16_bf16 v[156:171], v[120:123], v[64:67], v[156:171]\n"
      "v_mfma_f32_32x32x16_bf16 v[156:171], v[124:127], v[68:71], v[156:171]\n"
      "v_mfma_f32_32x32x16_bf16 v[156:171], v[128:131], v[72:75], v[156:171]\n"
      "v_mfma_f32_32x32x16_bf16 v[156:171], v[132:135], v[76:79], v[156:171]\n"
      "s_nop 10\n"
      "s_cmp_lt_u32 %[wid], 4\n"
      "s_cbranch_scc0 4f\n"
      ".p2align 6\n"
      "1:\n"
      "v_mfma_f32_32x32x16_bf16 v[16:31], v[152:155], v[180:183], v[16:31]\n"
      "ds_read_b64_tr_b16 v[148:149], v224 offset:2048\n"
      "v_exp_f32 v156, v156\n"
      "v_exp_f32 v157, v157\n"
      "v_exp_f32 v158, v158\n"
      "v_exp_f32 v159, v159\n"
      "v_mfma_f32_32x32x16_bf16 v[48:63], v[152:155], v[196:199], v[48:63]\n"
      "s_waitcnt lgkmcnt(1)\n"
      "ds_read_b64_tr_b16 v[150:151], v225 offset:2048\n"
      "v_exp_f32 v160, v160\n"
      "v_exp_f32 v161, v161\n"
      "v_exp_f32 v162, v162\n"
      "v_exp_f32 v163, v163\n"
      "v_mfma_f32_32x32x16_bf16 v[188:203], v[120:123], v[204:207], 0\n"
      "ds_read_b64_tr_b16 v[152:153], v226 offset:2048\n"
      "v_add_f32 v156, 1.0, v156\n"
      "v_add_f32 v157, 1.0, v157\n"
      "v_add_f32 v158, 1.0, v158\n"
      "v_add_f32 v159, 1.0, v159\n"
      "v_rcp_f32 v156, v156\n"
      "v_mfma_f32_32x32x16_bf16 v[188:203], v[124:127], v[208:211], v[188:203]\n"
      "ds_read_b64_tr_b16 v[154:155], v227 offset:2048\n"
      "v_rcp_f32 v157, v157\n"
      "v_rcp_f32 v158, v158\n"
      "v_rcp_f32 v159, v159\n"
      "v_add_f32 v160, 1.0, v160\n"
      "v_add_f32 v161, 1.0, v161\n"
      "v_mfma_f32_32x32x16_bf16 v[188:203], v[128:131], v[212:215], v[188:203]\n"
      "ds_read_b32 v229, v232 offset:128\n"
      "v_add_f32 v162, 1.0, v162\n"
      "v_add_f32 v163, 1.0, v163\n"
      "v_rcp_f32 v160, v160\n"
      "v_rcp_f32 v161, v161\n"
      "v_rcp_f32 v162, v162\n"
      "v_mfma_f32_32x32x16_bf16 v[188:203], v[132:135], v[216:219], v[188:203]\n"
      "ds_read_b128 v[224:227], v233 offset:128\n"
      "v_rcp_f32 v163, v163\n"
      "v_exp_f32 v164, v164\n"
      "v_exp_f32 v165, v165\n"
      "v_exp_f32 v166, v166\n"
      "v_mfma_f32_32x32x16_bf16 v[172:187], v[136:139], v[100:103], 0\n"
      "ds_read_b128 v[220:223], v230 offset:384\n"
      "v_exp_f32 v167, v167\n"
      "v_exp_f32 v168, v168\n"
      "v_exp_f32 v169, v169\n"
      "v_mfma_f32_32x32x16_bf16 v[172:187], v[140:143], v[112:115], v[172:187]\n"
      "ds_read_b32 v228, v231 offset:384\n"
      "v_exp_f32 v170, v170\n"
      "v_exp_f32 v171, v171\n"
      "v_mul_f32 v188, v156, v188\n"
      "v_mul_f32 v189, v157, v189\n"
      "v_mul_f32 v190, v158, v190\n"
      "v_mul_f32 v191, v159, v191\n"
      "v_mfma_f32_32x32x16_bf16 v[172:187], v[144:147], v[116:119], v[172:187]\n"
      "s_waitcnt vmcnt(5) lgkmcnt(3)\n"
      "ds_read_b128 v[136:139], v238 offset:40960\n"
      "v_mul_f32 v192, v160, v192\n"
      "v_mul_f32 v193, v161, v193\n"
      "v_mul_f32 v194, v162, v194\n"
      "v_and_or_b32 v229, v229, s84, v240\n"
      "v_mul_f32 v195, v163, v195\n"
      "v_cvt_pk_bf16_f32 v156, v156, v157\n"
      "v_cvt_pk_bf16_f32 v157, v158, v159\n"
      "v_cvt_pk_bf16_f32 v158, v160, v161\n"
      "v_mfma_f32_32x32x16_bf16 v[172:187], v[120:123], v[80:83], v[172:187]\n"
      "ds_read_b128 v[140:143], v229 offset:0\n"
      "ds_read_b128 v[144:147], v229 offset:32\n"
      "v_cvt_pk_bf16_f32 v159, v162, v163\n"
      "v_cvt_pk_bf16_f32 v188, v188, v189\n"
      "v_cvt_pk_bf16_f32 v189, v190, v191\n"
      "v_cvt_pk_bf16_f32 v190, v192, v193\n"
      "v_cvt_pk_bf16_f32 v191, v194, v195\n"
      "v_add_f32 v164, 1.0, v164\n"
      "v_add_f32 v165, 1.0, v165\n"
      "v_mfma_f32_32x32x16_bf16 v[172:187], v[124:127], v[84:87], v[172:187]\n"
      "ds_read_b128 v[204:207], v239 offset:4096\n"
      "v_add_f32 v166, 1.0, v166\n"
      "v_add_f32 v167, 1.0, v167\n"
      "v_rcp_f32 v164, v164\n"
      "v_rcp_f32 v165, v165\n"
      "v_rcp_f32 v166, v166\n"
      "v_mfma_f32_32x32x16_bf16 v[172:187], v[128:131], v[88:91], v[172:187]\n"
      "ds_read_b128 v[208:211], v239 offset:5120\n"
      "v_rcp_f32 v167, v167\n"
      "v_add_f32 v168, 1.0, v168\n"
      "v_add_f32 v169, 1.0, v169\n"
      "v_add_f32 v170, 1.0, v170\n"
      "v_add_f32 v171, 1.0, v171\n"
      "v_rcp_f32 v168, v168\n"
      "v_mfma_f32_32x32x16_bf16 v[172:187], v[132:135], v[92:95], v[172:187]\n"
      "ds_read_b128 v[212:215], v239 offset:6144\n"
      "v_rcp_f32 v169, v169\n"
      "v_rcp_f32 v170, v170\n"
      "v_rcp_f32 v171, v171\n"
      "v_mul_f32 v196, v164, v196\n"
      "v_mfma_f32_32x32x16_bf16 v[0:15], v[148:151], v[156:159], v[0:15]\n"
      "s_cmp_gt_i32 s87, 2\n"
      "ds_read_b128 v[216:219], v239 offset:7168\n"
      "v_mul_f32 v197, v165, v197\n"
      "v_mul_f32 v198, v166, v198\n"
      "v_mul_f32 v199, v167, v199\n"
      "v_mul_f32 v200, v168, v200\n"
      "v_mul_f32 v201, v169, v201\n"
      "v_mul_f32 v202, v170, v202\n"
      "v_mul_f32 v203, v171, v203\n"
      "v_mfma_f32_32x32x16_bf16 v[32:47], v[148:151], v[188:191], v[32:47]\n"
      "s_cselect_b64 s[88:89], %[xs1], %[wu1]\n"
      "v_cvt_pk_bf16_f32 v164, v164, v165\n"
      "v_cvt_pk_bf16_f32 v165, v166, v167\n"
      "v_cvt_pk_bf16_f32 v166, v168, v169\n"
      "v_cvt_pk_bf16_f32 v167, v170, v171\n"
      "v_cvt_pk_bf16_f32 v196, v196, v197\n"
      "v_cvt_pk_bf16_f32 v197, v198, v199\n"
      "v_cvt_pk_bf16_f32 v198, v200, v201\n"
      "v_cvt_pk_bf16_f32 v199, v202, v203\n"
      "v_mfma_f32_32x32x16_bf16 v[0:15], v[152:155], v[164:167], v[0:15]\n"
      "v_exp_f32 v172, v172\n"
      "v_exp_f32 v173, v173\n"
      "v_exp_f32 v174, v174\n"
      "v_exp_f32 v175, v175\n"
      "v_mfma_f32_32x32x16_bf16 v[32:47], v[152:155], v[196:199], v[32:47]\n"
      "s_waitcnt lgkmcnt(3)\n"
      "v_exp_f32 v176, v176\n"
      "v_exp_f32 v177, v177\n"
      "v_lshrrev_b32 v228, 11, v228\n"
      "v_exp_f32 v178, v178\n"
      "v_exp_f32 v179, v179\n"
      "v_mfma_f32_32x32x16_bf16 v[188:203], v[120:123], v[204:207], 0\n"
      "s_waitcnt lgkmcnt(2)\n"
      "ds_read_b128 v[120:123], v234 offset:40960\n"
      "v_add_f32 v172, 1.0, v172\n"
      "v_add_f32 v173, 1.0, v173\n"
      "v_lshl_add_u32 v228, v228, 5, v244\n"
      "v_add_f32 v174, 1.0, v174\n"
      "v_add_f32 v175, 1.0, v175\n"
      "v_rcp_f32 v172, v172\n"
      "v_mfma_f32_32x32x16_bf16 v[188:203], v[124:127], v[208:211], v[188:203]\n"
      "s_waitcnt lgkmcnt(2)\n"
      "ds_read_b128 v[124:127], v235 offset:40960\n"
      "v_rcp_f32 v173, v173\n"
      "v_rcp_f32 v174, v174\n"
      "v_mad_u32_u16 v220, v220, s85, v242 op_sel:[1,0,0,0]\n"
      "v_rcp_f32 v175, v175\n"
      "v_add_f32 v176, 1.0, v176\n"
      "v_add_f32 v177, 1.0, v177\n"
      "v_mfma_f32_32x32x16_bf16 v[188:203], v[128:131], v[212:215], v[188:203]\n"
      "s_waitcnt lgkmcnt(2)\n"
      "ds_read_b128 v[128:131], v236 offset:40960\n"
      "v_add_f32 v178, 1.0, v178\n"
      "v_add_f32 v179, 1.0, v179\n"
      "v_mad_u32_u16 v221, v221, s85, v243 op_sel:[1,0,0,0]\n"
      "v_rcp_f32 v176, v176\n"
      "v_rcp_f32 v177, v177\n"
      "v_mfma_f32_32x32x16_bf16 v[188:203], v[132:135], v[216:219], v[188:203]\n"
      "ds_read_b128 v[132:135], v237 offset:40960\n"
      "v_rcp_f32 v178, v178\n"
      "v_rcp_f32 v179, v179\n"
      "v_mad_u32_u16 v222, v222, s85, v242 op_sel:[1,0,0,0]\n"
      "v_exp_f32 v180, v180\n"
      "v_exp_f32 v181, v181\n"
      "v_mfma_f32_32x32x16_bf16 v[156:171], v[140:143], v[104:107], 0\n"
      "v_exp_f32 v182, v182\n"
      "v_mad_u32_u16 v223, v223, s85, v243 op_sel:[1,0,0,0]\n"
      "v_exp_f32 v183, v183\n"
      "v_exp_f32 v184, v184\n"
      "v_mfma_f32_32x32x16_bf16 v[156:171], v[144:147], v[108:111], v[156:171]\n"
      "s_add_u32 m0, %[stg], 45056\n"
      "v_exp_f32 v185, v185\n"
      "v_exp_f32 v186, v186\n"
      "v_and_or_b32 v224, v224, s84, v241\n"
      "v_exp_f32 v187, v187\n"
      "v_mul_f32 v188, v172, v188\n"
      "v_mul_f32 v189, v173, v189\n"
      "v_mfma_f32_32x32x16_bf16 v[156:171], v[136:139], v[96:99], v[156:171]\n"
      "s_waitcnt lgkmcnt(3)\n"
      "v_mul_f32 v190, v174, v190\n"
      "v_mul_f32 v191, v175, v191\n"
      "v_mul_f32 v192, v176, v192\n"
      "v_and_or_b32 v225, v225, s84, v241\n"
      "v_mul_f32 v193, v177, v193\n"
      "v_mul_f32 v194, v178, v194\n"
      "v_mul_f32 v195, v179, v195\n"
      "v_cvt_pk_bf16_f32 v172, v172, v173\n"
      "v_mfma_f32_32x32x16_bf16 v[156:171], v[120:123], v[64:67], v[156:171]\n"
      "s_waitcnt lgkmcnt(0)\n"
      "global_load_lds_dwordx4 v228, %[ea] sc1\n"
      "ds_read_b128 v[204:207], v239 offset:0\n"
      "v_cvt_pk_bf16_f32 v173, v174, v175\n"
      "v_cvt_pk_bf16_f32 v174, v176, v177\n"
      "v_cvt_pk_bf16_f32 v175, v178, v179\n"
      "v_and_or_b32 v226, v226, s84, v241\n"
      "v_cvt_pk_bf16_f32 v188, v188, v189\n"
      "v_cvt_pk_bf16_f32 v189, v190, v191\n"
      "v_cvt_pk_bf16_f32 v190, v192, v193\n"
      "v_cvt_pk_bf16_f32 v191, v194, v195\n"
      "v_mfma_f32_32x32x16_bf16 v[156:171], v[124:127], v[68:71], v[156:171]\n"
      "s_add_u32 m0, %[stg], 40960\n"
      "ds_read_b128 v[208:211], v239 offset:1024\n"
      "v_add_f32 v180, 1.0, v180\n"
      "v_add_f32 v181, 1.0, v181\n"
      "v_add_f32 v182, 1.0, v182\n"
      "v_and_or_b32 v227, v227, s84, v241\n"
      "v_add_f32 v183, 1.0, v183\n"
      "v_rcp_f32 v180, v180\n"
      "v_rcp_f32 v181, v181\n"
      "v_mfma_f32_32x32x16_bf16 v[156:171], v[128:131], v[72:75], v[156:171]\n"
      "global_load_lds_dwordx4 v220, s[88:89]\n"
      "ds_read_b128 v[212:215], v239 offset:2048\n"
      "v_rcp_f32 v182, v182\n"
      "v_rcp_f32 v183, v183\n"
      "v_add_f32 v184, 1.0, v184\n"
      "v_add_f32 v185, 1.0, v185\n"
      "v_add_f32 v186, 1.0, v186\n"
      "v_add_f32 v187, 1.0, v187\n"
      "v_mfma_f32_32x32x16_bf16 v[156:171], v[132:135], v[76:79], v[156:171]\n"
      "global_load_lds_dwordx4 v221, s[88:89] offset:1024\n"
      "ds_read_b128 v[216:219], v239 offset:3072\n"
      "v_rcp_f32 v184, v184\n"
      "v_rcp_f32 v185, v185\n"
      "v_rcp_f32 v186, v186\n"
      "v_rcp_f32 v187, v187\n"
      "v_mfma_f32_32x32x16_bf16 v[16:31], v[148:151], v[172:175], v[16:31]\n"
      "s_add_u32 m0, %[stg], 43008\n"
      "v_mul_f32 v196, v180, v196\n"
      "v_mul_f32 v197, v181, v197\n"
      "v_mul_f32 v198, v182, v198\n"
      "v_mul_f32 v199, v183, v199\n"
      "v_mul_f32 v200, v184, v200\n"
      "v_mul_f32 v201, v185, v201\n"
      "v_mul_f32 v202, v186, v202\n"
      "v_mul_f32 v203, v187, v203\n"
      "v_mfma_f32_32x32x16_bf16 v[48:63], v[148:151], v[188:191], v[48:63]\n"
      "s_sub_u32 s87, s87, 1\n"
      "global_load_lds_dwordx4 v222, s[88:89]\n"
      "global_load_lds_dwordx4 v223, s[88:89] offset:1024\n"
      "v_cvt_pk_bf16_f32 v180, v180, v181\n"
      "v_cvt_pk_bf16_f32 v181, v182, v183\n"
      "v_cvt_pk_bf16_f32 v182, v184, v185\n"
      "v_cvt_pk_bf16_f32 v183, v186, v187\n"
      "v_cvt_pk_bf16_f32 v196, v196, v197\n"
      "v_cvt_pk_bf16_f32 v197, v198, v199\n"
      "v_cvt_pk_bf16_f32 v198, v200, v201\n"
      "v_cvt_pk_bf16_f32 v199, v202, v203\n"
      "s_cbranch_scc1 2f\n"
      "v_mfma_f32_32x32x16_bf16 v[16:31], v[152:155], v[180:183], v[16:31]\n"
      "ds_read_b64_tr_b16 v[148:149], v224 offset:2048\n"
      "v_exp_f32 v156, v156\n"
      "v_exp_f32 v157, v157\n"
      "v_exp_f32 v158, v158\n"
      "v_exp_f32 v159, v159\n"
      "v_mfma_f32_32x32x16_bf16 v[48:63], v[152:155], v[196:199], v[48:63]\n"
      "s_waitcnt lgkmcnt(1)\n"
      "ds_read_b64_tr_b16 v[150:151], v225 offset:2048\n"
      "v_exp_f32 v160, v160\n"
      "v_exp_f32 v161, v161\n"
      "v_exp_f32 v162, v162\n"
      "v_exp_f32 v163, v163\n"
      "v_mfma_f32_32x32x16_bf16 v[188:203], v[120:123], v[204:207], 0\n"
      "ds_read_b64_tr_b16 v[152:153], v226 offset:2048\n"
      "v_add_f32 v156, 1.0, v156\n"
      "v_add_f32 v157, 1.0, v157\n"
      "v_add_f32 v158, 1.0, v158\n"
      "v_add_f32 v159, 1.0, v159\n"
      "v_rcp_f32 v156, v156\n"
      "v_rcp_f32 v157, v157\n"
      "v_mfma_f32_32x32x16_bf16 v[188:203], v[124:127], v[208:211], v[188:203]\n"
      "ds_read_b64_tr_b16 v[154:155], v227 offset:2048\n"
      "v_rcp_f32 v158, v158\n"
      "v_rcp_f32 v159, v159\n"
      "v_add_f32 v160, 1.0, v160\n"
      "v_add_f32 v161, 1.0, v161\n"
      "v_add_f32 v162, 1.0, v162\n"
      "v_mfma_f32_32x32x16_bf16 v[188:203], v[128:131], v[212:215], v[188:203]\n"
      "ds_read_b32 v229, v232 offset:256\n"
      "v_add_f32 v163, 1.0, v163\n"
      "v_rcp_f32 v160, v160\n"
      "v_rcp_f32 v161, v161\n"
      "v_rcp_f32 v162, v162\n"
      "v_rcp_f32 v163, v163\n"
      "v_mfma_f32_32x32x16_bf16 v[188:203], v[132:135], v[216:219], v[188:203]\n"
      "ds_read_b128 v[224:227], v233 offset:256\n"
      "v_exp_f32 v164, v164\n"
      "v_add_u32 v232, s86, v232\n"
      "v_exp_f32 v165, v165\n"
      "v_exp_f32 v166, v166\n"
      "v_mfma_f32_32x32x16_bf16 v[172:187], v[136:139], v[100:103], 0\n"
      "ds_read_b128 v[220:223], v230 offset:512\n"
      "v_exp_f32 v167, v167\n"
      "v_add_u32 v233, s86, v233\n"
      "v_exp_f32 v168, v168\n"
      "v_exp_f32 v169, v169\n"
      "v_mfma_f32_32x32x16_bf16 v[172:187], v[140:143], v[112:115], v[172:187]\n"
      "ds_read_b32 v228, v231 offset:512\n"
      "v_exp_f32 v170, v170\n"
      "v_exp_f32 v171, v171\n"
      "v_add_u32 v230, s86, v230\n"
      "v_mul_f32 v188, v156, v188\n"
      "v_mul_f32 v189, v157, v189\n"
      "v_mul_f32 v190, v158, v190\n"
      "v_mfma_f32_32x32x16_bf16 v[172:187], v[144:147], v[116:119], v[172:187]\n"
      "s_waitcnt vmcnt(5) lgkmcnt(3)\n"
      "ds_read_b128 v[136:139], v238 offset:0\n"
      "v_mul_f32 v191, v159, v191\n"
      "v_mul_f32 v192, v160, v192\n"
      "v_mul_f32 v193, v161, v193\n"
      "v_and_or_b32 v229, v229, s84, v240\n"
      "v_mul_f32 v194, v162, v194\n"
      "v_mul_f32 v195, v163, v195\n"
      "v_cvt_pk_bf16_f32 v156, v156, v157\n"
      "v_cvt_pk_bf16_f32 v157, v158, v159\n"
      "v_mfma_f32_32x32x16_bf16 v[172:187], v[120:123], v[80:83], v[172:187]\n"
      "ds_read_b128 v[140:143], v229 offset:0\n"
      "ds_read_b128 v[144:147], v229 offset:32\n"
      "v_cvt_pk_bf16_f32 v158, v160, v161\n"
      "v_cvt_pk_bf16_f32 v159, v162, v163\n"
      "v_cvt_pk_bf16_f32 v188, v188, v189\n"
      "v_add_u32 v231, s86, v231\n"
      "v_cvt_pk_bf16_f32 v189, v190, v191\n"
      "v_cvt_pk_bf16_f32 v190, v192, v193\n"
      "v_cvt_pk_bf16_f32 v191, v194, v195\n"
      "v_add_f32 v164, 1.0, v164\n"
      "v_mfma_f32_32x32x16_bf16 v[172:187], v[124:127], v[84:87], v[172:187]\n"
      "ds_read_b128 v[204:207], v239 offset:4096\n"
      "v_add_f32 v165, 1.0, v165\n"
      "v_add_f32 v166, 1.0, v166\n"
      "v_add_f32 v167, 1.0, v167\n"
      "v_rcp_f32 v164, v164\n"
      "v_rcp_f32 v165, v165\n"
      "v_mfma_f32_32x32x16_bf16 v[172:187], v[128:131], v[88:91], v[172:187]\n"
      "ds_read_b128 v[208:211], v239 offset:5120\n"
      "v_rcp_f32 v166, v166\n"
      "v_rcp_f32 v167, v167\n"
      "v_add_f32 v168, 1.0, v168\n"
      "v_add_f32 v169, 1.0, v169\n"
      "v_add_f32 v170, 1.0, v170\n"
      "v_add_f32 v171, 1.0, v171\n"
      "v_mfma_f32_32x32x16_bf16 v[172:187], v[132:135], v[92:95], v[172:187]\n"
      "ds_read_b128 v[212:215], v239 offset:6144\n"
      "v_rcp_f32 v168, v168\n"
      "v_rcp_f32 v169, v169\n"
      "v_rcp_f32 v170, v170\n"
      "v_rcp_f32 v171, v171\n"
      "v_mfma_f32_32x32x16_bf16 v[0:15], v[148:151], v[156:159], v[0:15]\n"
      "s_cmp_gt_i32 s87, 2\n"
      "ds_read_b128 v[216:219], v239 offset:7168\n"
      "v_mul_f32 v196, v164, v196\n"
      "v_mul_f32 v197, v165, v197\n"
      "v_mul_f32 v198, v166, v198\n"
      "v_mul_f32 v199, v167, v199\n"
      "v_mul_f32 v200, v168, v200\n"
      "v_mul_f32 v201, v169, v201\n"
      "v_mul_f32 v202, v170, v202\n"
      "v_mul_f32 v203, v171, v203\n"
      "v_mfma_f32_32x32x16_bf16 v[32:47], v[148:151], v[188:191], v[32:47]\n"
      "s_cselect_b64 s[88:89], %[xs1], %[wu1]\n"
      "v_cvt_pk_bf16_f32 v164, v164, v165\n"
      "v_cvt_pk_bf16_f32 v165, v166, v167\n"
      "v_cvt_pk_bf16_f32 v166, v168, v169\n"
      "v_cvt_pk_bf16_f32 v167, v170, v171\n"
      "v_cvt_pk_bf16_f32 v196, v196, v197\n"
      "v_cvt_pk_bf16_f32 v197, v198, v199\n"
      "v_cvt_pk_bf16_f32 v198, v200, v201\n"
      "v_cvt_pk_bf16_f32 v199, v202, v203\n"
      "v_mfma_f32_32x32x16_bf16 v[0:15], v[152:155], v[164:167], v[0:15]\n"
      "v_exp_f32 v172, v172\n"
      "v_exp_f32 v173, v173\n"
      "v_exp_f32 v174, v174\n"
      "v_exp_f32 v175, v175\n"
      "v_mfma_f32_32x32x16_bf16 v[32:47], v[152:155], v[196:199], v[32:47]\n"
      "s_waitcnt lgkmcnt(3)\n"
      "v_exp_f32 v176, v176\n"
      "v_exp_f32 v177, v177\n"
      "v_lshrrev_b32 v228, 11, v228\n"
      "v_exp_f32 v178, v178\n"
      "v_exp_f32 v179, v179\n"
      "v_mfma_f32_32x32x16_bf16 v[188:203], v[120:123], v[204:207], 0\n"
      "s_waitcnt lgkmcnt(2)\n"
      "ds_read_b128 v[120:123], v234 offset:0\n"
      "v_add_f32 v172, 1.0, v172\n"
      "v_add_f32 v173, 1.0, v173\n"
      "v_lshl_add_u32 v228, v228, 5, v244\n"
      "v_add_f32 v174, 1.0, v174\n"
      "v_add_f32 v175, 1.0, v175\n"
      "v_rcp_f32 v172, v172\n"
      "v_mfma_f32_32x32x16_bf16 v[188:203], v[124:127], v[208:211], v[188:203]\n"
      "s_waitcnt lgkmcnt(2)\n"
      "ds_read_b128 v[124:127], v235 offset:0\n"
      "v_rcp_f32 v173, v173\n"
      "v_rcp_f32 v174, v174\n"
      "v_mad_u32_u16 v220, v220, s85, v242 op_sel:[1,0,0,0]\n"
      "v_rcp_f32 v175, v175\n"
      "v_add_f32 v176, 1.0, v176\n"
      "v_add_f32 v177, 1.0, v177\n"
      "v_mfma_f32_32x32x16_bf16 v[188:203], v[128:131], v[212:215], v[188:203]\n"
      "s_waitcnt lgkmcnt(2)\n"
      "ds_read_b128 v[128:131], v236 offset:0\n"
      "v_add_f32 v178, 1.0, v178\n"
      "v_add_f32 v179, 1.0, v179\n"
      "v_mad_u32_u16 v221, v221, s85, v243 op_sel:[1,0,0,0]\n"
      "v_rcp_f32 v176, v176\n"
      "v_rcp_f32 v177, v177\n"
      "v_mfma_f32_32x32x16_bf16 v[188:203], v[132:135], v[216:219], v[188:203]\n"
      "ds_read_b128 v[132:135], v237 offset:0\n"
      "v_rcp_f32 v178, v178\n"
      "v_rcp_f32 v179, v179\n"
      "v_mad_u32_u16 v222, v222, s85, v242 op_sel:[1,0,0,0]\n"
      "v_exp_f32 v180, v180\n"
      "v_exp_f32 v181, v181\n"
      "v_mfma_f32_32x32x16_bf16 v[156:171], v[140:143], v[104:107], 0\n"
      "v_exp_f32 v182, v182\n"
      "v_mad_u32_u16 v223, v223, s85, v243 op_sel:[1,0,0,0]\n"
      "v_exp_f32 v183, v183\n"
      "v_exp_f32 v184, v184\n"
      "v_mfma_f32_32x32x16_bf16 v[156:171], v[144:147], v[108:111], v[156:171]\n"
      "s_add_u32 m0, %[stg], 4096\n"
      "v_exp_f32 v185, v185\n"
      "v_exp_f32 v186, v186\n"
      "v_and_or_b32 v224, v224, s84, v241\n"
      "v_exp_f32 v187, v187\n"
      "v_mul_f32 v188, v172, v188\n"
      "v_mul_f32 v189, v173, v189\n"
      "v_mfma_f32_32x32x16_bf16 v[156:171], v[136:139], v[96:99], v[156:171]\n"
      "s_waitcnt lgkmcnt(3)\n"
      "v_mul_f32 v190, v174, v190\n"
      "v_mul_f32 v191, v175, v191\n"
      "v_mul_f32 v192, v176, v192\n"
      "v_and_or_b32 v225, v225, s84, v241\n"
      "v_mul_f32 v193, v177, v193\n"
      "v_mul_f32 v194, v178, v194\n"
      "v_mul_f32 v195, v179, v195\n"
      "v_cvt_pk_bf16_f32 v172, v172, v173\n"
      "v_mfma_f32_32x32x16_bf16 v[156:171], v[120:123], v[64:67], v[156:171]\n"
      "s_waitcnt lgkmcnt(0)\n"
      "global_load_lds_dwordx4 v228, %[ea] sc1\n"
      "ds_read_b128 v[204:207], v239 offset:0\n"
      "v_cvt_pk_bf16_f32 v173, v174, v175\n"
      "v_cvt_pk_bf16_f32 v174, v176, v177\n"
      "v_cvt_pk_bf16_f32 v175, v178, v179\n"
      "v_and_or_b32 v226, v226, s84, v241\n"
      "v_cvt_pk_bf16_f32 v188, v188, v189\n"
      "v_cvt_pk_bf16_f32 v189, v190, v191\n"
      "v_cvt_pk_bf16_f32 v190, v192, v193\n"
      "v_cvt_pk_bf16_f32 v191, v194, v195\n"
      "v_mfma_f32_32x32x16_bf16 v[156:171], v[124:127], v[68:71], v[156:171]\n"
      "s_mov_b32 m0, %[stg]\n"
      "ds_read_b128 v[208:211], v239 offset:1024\n"
      "v_add_f32 v180, 1.0, v180\n"
      "v_add_f32 v181, 1.0, v181\n"
      "v_add_f32 v182, 1.0, v182\n"
      "v_and_or_b32 v227, v227, s84, v241\n"
      "v_add_f32 v183, 1.0, v183\n"
      "v_rcp_f32 v180, v180\n"
      "v_rcp_f32 v181, v181\n"
      "v_mfma_f32_32x32x16_bf16 v[156:171], v[128:131], v[72:75], v[156:171]\n"
      "global_load_lds_dwordx4 v220, s[88:89]\n"
      "ds_read_b128 v[212:215], v239 offset:2048\n"
      "v_rcp_f32 v182, v182\n"
      "v_rcp_f32 v183, v183\n"
      "v_add_f32 v184, 1.0, v184\n"
      "v_add_f32 v185, 1.0, v185\n"
      "v_add_f32 v186, 1.0, v186\n"
      "v_add_f32 v187, 1.0, v187\n"
      "v_mfma_f32_32x32x16_bf16 v[156:171], v[132:135], v[76:79], v[156:171]\n"
      "global_load_lds_dwordx4 v221, s[88:89] offset:1024\n"
      "ds_read_b128 v[216:219], v239 offset:3072\n"
      "v_rcp_f32 v184, v184\n"
      "v_rcp_f32 v185, v185\n"
      "v_rcp_f32 v186, v186\n"
      "v_rcp_f32 v187, v187\n"
      "v_mfma_f32_32x32x16_bf16 v[16:31], v[148:151], v[172:175], v[16:31]\n"
      "s_add_u32 m0, %[stg], 2048\n"
      "v_mul_f32 v196, v180, v196\n"
      "v_mul_f32 v197, v181, v197\n"
      "v_mul_f32 v198, v182, v198\n"
      "v_mul_f32 v199, v183, v199\n"
      "v_mul_f32 v200, v184, v200\n"
      "v_mul_f32 v201, v185, v201\n"
      "v_mul_f32 v202, v186, v202\n"
      "v_mul_f32 v203, v187, v203\n"
      "v_mfma_f32_32x32x16_bf16 v[48:63], v[148:151], v[188:191], v[48:63]\n"
      "s_sub_u32 s87, s87, 1\n"
      "global_load_lds_dwordx4 v222, s[88:89]\n"
      "global_load_lds_dwordx4 v223, s[88:89] offset:1024\n"
      "v_cvt_pk_bf16_f32 v180, v180, v181\n"
      "v_cvt_pk_bf16_f32 v181, v182, v183\n"
      "v_cvt_pk_bf16_f32 v182, v184, v185\n"
      "v_cvt_pk_bf16_f32 v183, v186, v187\n"
      "v_cvt_pk_bf16_f32 v196, v196, v197\n"
      "v_cvt_pk_bf16_f32 v197, v198, v199\n"
      "v_cvt_pk_bf16_f32 v198, v200, v201\n"
      "v_cvt_pk_bf16_f32 v199, v202, v203\n"
      "s_cbranch_scc0 1b\n"
      "s_branch 2f\n"
      ".p2align 6\n"
      "4:\n"
      "s_setprio 1\n"
      "v_mfma_f32_32x32x16_bf16 v[16:31], v[152:155], v[180:183], v[16:31]\n"
      "ds_read_b64_tr_b16 v[148:149], v224 offset:2048\n"
      "v_exp_f32 v156, v156\n"
      "v_exp_f32 v157, v157\n"
      "v_exp_f32 v158, v158\n"
      "v_exp_f32 v159, v159\n"
      "v_mfma_f32_32x32x16_bf16 v[48:63], v[152:155], v[196:199], v[48:63]\n"
      "s_waitcnt lgkmcnt(1)\n"
      "ds_read_b64_tr_b16 v[150:151], v225 offset:2048\n"
      "v_exp_f32 v160, v160\n"
      "v_exp_f32 v161, v161\n"
      "v_exp_f32 v162, v162\n"
      "v_exp_f32 v163, v163\n"
      "v_mfma_f32_32x32x16_bf16 v[188:203], v[120:123], v[204:207], 0\n"
      "ds_read_b64_tr_b16 v[152:153], v226 offset:2048\n"
      "v_add_f32 v156, 1.0, v156\n"
      "v_add_f32 v157, 1.0, v157\n"
      "v_add_f32 v158, 1.0, v158\n"
      "v_add_f32 v159, 1.0, v159\n"
      "v_rcp_f32 v156, v156\n"
      "v_mfma_f32_32x32x16_bf16 v[188:203], v[124:127], v[208:211], v[188:203]\n"
      "ds_read_b64_tr_b16 v[154:155], v227 offset:2048\n"
      "v_rcp_f32 v157, v157\n"
      "v_rcp_f32 v158, v158\n"
      "v_rcp_f32 v159, v159\n"
      "v_add_f32 v160, 1.0, v160\n"
      "v_add_f32 v161, 1.0, v161\n"
      "v_mfma_f32_32x32x16_bf16 v[188:203], v[128:131], v[212:215], v[188:203]\n"
      "ds_read_b32 v229, v232 offset:128\n"
      "v_add_f32 v162, 1.0, v162\n"
      "v_add_f32 v163, 1.0, v163\n"
      "v_rcp_f32 v160, v160\n"
      "v_rcp_f32 v161, v161\n"
      "v_rcp_f32 v162, v162\n"
      "v_mfma_f32_32x32x16_bf16 v[188:203], v[132:135], v[216:219], v[188:203]\n"
      "ds_read_b128 v[224:227], v233 offset:128\n"
      "v_rcp_f32 v163, v163\n"
      "v_exp_f32 v164, v164\n"
      "v_exp_f32 v165, v165\n"
      "v_exp_f32 v166, v166\n"
      "v_mfma_f32_32x32x16_bf16 v[172:187], v[136:139], v[100:103], 0\n"
      "ds_read_b128 v[220:223], v230 offset:384\n"
      "v_exp_f32 v167, v167\n"
      "v_exp_f32 v168, v168\n"
      "v_exp_f32 v169, v169\n"
      "v_mfma_f32_32x32x16_bf16 v[172:187], v[140:143], v[112:115], v[172:187]\n"
      "ds_read_b32 v228, v231 offset:384\n"
      "v_exp_f32 v170, v170\n"
      "v_exp_f32 v171, v171\n"
      "v_mul_f32 v188, v156, v188\n"
      "v_mul_f32 v189, v157, v189\n"
      "v_mul_f32 v190, v158, v190\n"
      "v_mul_f32 v191, v159, v191\n"
      "v_mfma_f32_32x32x16_bf16 v[172:187], v[144:147], v[116:119], v[172:187]\n"
      "s_waitcnt vmcnt(5) lgkmcnt(3)\n"
      "ds_read_b128 v[136:139], v238 offset:40960\n"
      "v_mul_f32 v192, v160, v192\n"
      "v_mul_f32 v193, v161, v193\n"
      "v_mul_f32 v194, v162, v194\n"
      "v_and_or_b32 v229, v229, s84, v240\n"
      "v_mul_f32 v195, v163, v195\n"
      "v_cvt_pk_bf16_f32 v156, v156, v157\n"
      "v_cvt_pk_bf16_f32 v157, v158, v159\n"
      "v_cvt_pk_bf16_f32 v158, v160, v161\n"
      "v_mfma_f32_32x32x16_bf16 v[172:187], v[120:123], v[80:83], v[172:187]\n"
      "ds_read_b128 v[140:143], v229 offset:0\n"
      "ds_read_b128 v[144:147], v229 offset:32\n"
      "v_cvt_pk_bf16_f32 v159, v162, v163\n"
      "v_cvt_pk_bf16_f32 v188, v188, v189\n"
      "v_cvt_pk_bf16_f32 v189, v190, v191\n"
      "v_cvt_pk_bf16_f32 v190, v192, v193\n"
      "v_cvt_pk_bf16_f32 v191, v194, v195\n"
      "v_add_f32 v164, 1.0, v164\n"
      "v_add_f32 v165, 1.0, v165\n"
      "v_mfma_f32_32x32x16_bf16 v[172:187], v[124:127], v[84:87], v[172:187]\n"
      "ds_read_b128 v[204:207], v239 offset:4096\n"
      "v_add_f32 v166, 1.0, v166\n"
      "v_add_f32 v167, 1.0, v167\n"
      "v_rcp_f32 v164, v164\n"
      "v_rcp_f32 v165, v165\n"
      "v_rcp_f32 v166, v166\n"
      "v_mfma_f32_32x32x16_bf16 v[172:187], v[128:131], v[88:91], v[172:187]\n"
      "ds_read_b128 v[208:211], v239 offset:5120\n"
      "v_rcp_f32 v167, v167\n"
      "v_add_f32 v168, 1.0, v168\n"
      "v_add_f32 v169, 1.0, v169\n"
      "v_add_f32 v170, 1.0, v170\n"
      "v_add_f32 v171, 1.0, v171\n"
      "v_rcp_f32 v168, v168\n"
      "v_mfma_f32_32x32x16_bf16 v[172:187], v[132:135], v[92:95], v[172:187]\n"
      "ds_read_b128 v[212:215], v239 offset:6144\n"
      "v_rcp_f32 v169, v169\n"
      "v_rcp_f32 v170, v170\n"
      "v_rcp_f32 v171, v171\n"
      "v_mul_f32 v196, v164, v196\n"
      "v_mfma_f32_32x32x16_bf16 v[0:15], v[148:151], v[156:159], v[0:15]\n"
      "s_cmp_gt_i32 s87, 2\n"
      "ds_read_b128 v[216:219], v239 offset:7168\n"
      "v_mul_f32 v197, v165, v197\n"
      "v_mul_f32 v198, v166, v198\n"
      "v_mul_f32 v199, v167, v199\n"
      "v_mul_f32 v200, v168, v200\n"
      "v_mul_f32 v201, v169, v201\n"
      "v_mul_f32 v202, v170, v202\n"
      "v_mul_f32 v203, v171, v203\n"
      "v_mfma_f32_32x32x16_bf16 v[32:47], v[148:151], v[188:191], v[32:47]\n"
      "s_cselect_b64 s[88:89], %[xs1], %[wu1]\n"
      "v_cvt_pk_bf16_f32 v164, v164, v165\n"
      "v_cvt_pk_bf16_f32 v165, v166, v167\n"
      "v_cvt_pk_bf16_f32 v166, v168, v169\n"
      "v_cvt_pk_bf16_f32 v167, v170, v171\n"
      "v_cvt_pk_bf16_f32 v196, v196, v197\n"
      "v_cvt_pk_bf16_f32 v197, v198, v199\n"
      "v_cvt_pk_bf16_f32 v198, v200, v201\n"
      "v_cvt_pk_bf16_f32 v199, v202, v203\n"
      "v_mfma_f32_32x32x16_bf16 v[0:15], v[152:155], v[164:167], v[0:15]\n"
      "v_exp_f32 v172, v172\n"
      "v_exp_f32 v173, v173\n"
      "v_exp_f32 v174, v174\n"
      "v_exp_f32 v175, v175\n"
      "v_mfma_f32_32x32x16_bf16 v[32:47], v[152:155], v[196:199], v[32:47]\n"
      "s_waitcnt lgkmcnt(3)\n"
      "v_exp_f32 v176, v176\n"
      "v_exp_f32 v177, v177\n"
      "v_lshrrev_b32 v228, 11, v228\n"
      "v_exp_f32 v178, v178\n"
      "v_exp_f32 v179, v179\n"
      "v_mfma_f32_32x32x16_bf16 v[188:203], v[120:123], v[204:207], 0\n"
      "s_waitcnt lgkmcnt(2)\n"
      "ds_read_b128 v[120:123], v234 offset:40960\n"
      "v_add_f32 v172, 1.0, v172\n"
      "v_add_f32 v173, 1.0, v173\n"
      "v_lshl_add_u32 v228, v228, 5, v244\n"
      "v_add_f32 v174, 1.0, v174\n"
      "v_add_f32 v175, 1.0, v175\n"
      "v_rcp_f32 v172, v172\n"
      "v_mfma_f32_32x32x16_bf16 v[188:203], v[124:127], v[208:211], v[188:203]\n"
      "s_waitcnt lgkmcnt(2)\n"
      "ds_read_b128 v[124:127], v235 offset:40960\n"
      "v_rcp_f32 v173, v173\n"
      "v_rcp_f32 v174, v174\n"
      "v_mad_u32_u16 v220, v220, s85, v242 op_sel:[1,0,0,0]\n"
      "v_rcp_f32 v175, v175\n"
      "v_add_f32 v176, 1.0, v176\n"
      "v_add_f32 v177, 1.0, v177\n"
      "v_mfma_f32_32x32x16_bf16 v[188:203], v[128:131], v[212:215], v[188:203]\n"
      "s_waitcnt lgkmcnt(2)\n"
      "ds_read_b128 v[128:131], v236 offset:40960\n"
      "v_add_f32 v178, 1.0, v178\n"
      "v_add_f32 v179, 1.0, v179\n"
      "v_mad_u32_u16 v221, v221, s85, v243 op_sel:[1,0,0,0]\n"
      "v_rcp_f32 v176, v176\n"
      "v_rcp_f32 v177, v177\n"
      "v_mfma_f32_32x32x16_bf16 v[188:203], v[132:135], v[216:219], v[188:203]\n"
      "ds_read_b128 v[132:135], v237 offset:40960\n"
      "v_rcp_f32 v178, v178\n"
      "v_rcp_f32 v179, v179\n"
      "v_mad_u32_u16 v222, v222, s85, v242 op_sel:[1,0,0,0]\n"
      "v_exp_f32 v180, v180\n"
      "v_exp_f32 v181, v181\n"
      "v_mfma_f32_32x32x16_bf16 v[156:171], v[140:143], v[104:107], 0\n"
      "v_exp_f32 v182, v182\n"
      "v_mad_u32_u16 v223, v223, s85, v243 op_sel:[1,0,0,0]\n"
      "v_exp_f32 v183, v183\n"
      "v_exp_f32 v184, v184\n"
      "v_mfma_f32_32x32x16_bf16 v[156:171], v[144:147], v[108:111], v[156:171]\n"
      "s_add_u32 m0, %[stg], 45056\n"
      "v_exp_f32 v185, v185\n"
      "v_exp_f32 v186, v186\n"
      "v_and_or_b32 v224, v224, s84, v241\n"
      "v_exp_f32 v187, v187\n"
      "v_mul_f32 v188, v172, v188\n"
      "v_mul_f32 v189, v173, v189\n"
      "v_mfma_f32_32x32x16_bf16 v[156:171], v[136:139], v[96:99], v[156:171]\n"
      "s_waitcnt lgkmcnt(3)\n"
      "v_mul_f32 v190, v174, v190\n"
      "v_mul_f32 v191, v175, v191\n"
      "v_mul_f32 v192, v176, v192\n"
      "v_and_or_b32 v225, v225, s84, v241\n"
      "v_mul_f32 v193, v177, v193\n"
      "v_mul_f32 v194, v178, v194\n"
      "v_mul_f32 v195, v179, v195\n"
      "v_cvt_pk_bf16_f32 v172, v172, v173\n"
      "v_mfma_f32_32x32x16_bf16 v[156:171], v[120:123], v[64:67], v[156:171]\n"
      "s_waitcnt lgkmcnt(0)\n"
      "global_load_lds_dwordx4 v228, %[ea] sc1\n"
      "ds_read_b128 v[204:207], v239 offset:0\n"
      "v_cvt_pk_bf16_f32 v173, v174, v175\n"
      "v_cvt_pk_bf16_f32 v174, v176, v177\n"
      "v_cvt_pk_bf16_f32 v175, v178, v179\n"
      "v_and_or_b32 v226, v226, s84, v241\n"
      "v_cvt_pk_bf16_f32 v188, v188, v189\n"
      "v_cvt_pk_bf16_f32 v189, v190, v191\n"
      "v_cvt_pk_bf16_f32 v190, v192, v193\n"
      "v_cvt_pk_bf16_f32 v191, v194, v195\n"
      "v_mfma_f32_32x32x16_bf16 v[156:171], v[124:127], v[68:71], v[156:171]\n"
      "s_add_u32 m0, %[stg], 40960\n"
      "ds_read_b128 v[208:211], v239 offset:1024\n"
      "v_add_f32 v180, 1.0, v180\n"
      "v_add_f32 v181, 1.0, v181\n"
      "v_add_f32 v182, 1.0, v182\n"
      "v_and_or_b32 v227, v227, s84, v241\n"
      "v_add_f32 v183, 1.0, v183\n"
      "v_rcp_f32 v180, v180\n"
      "v_rcp_f32 v181, v181\n"
      "s_setprio 0\n"
      "v_mfma_f32_32x32x16_bf16 v[156:171], v[128:131], v[72:75], v[156:171]\n"
      "global_load_lds_dwordx4 v220, s[88:89]\n"
      "ds_read_b128 v[212:215], v239 offset:2048\n"
      "v_rcp_f32 v182, v182\n"
      "v_rcp_f32 v183, v183\n"
      "v_add_f32 v184, 1.0, v184\n"
      "v_add_f32 v185, 1.0, v185\n"
      "v_add_f32 v186, 1.0, v186\n"
      "v_add_f32 v187, 1.0, v187\n"
      "v_mfma_f32_32x32x16_bf16 v[156:171], v[132:135], v[76:79], v[156:171]\n"
      "global_load_lds_dwordx4 v221, s[88:89] offset:1024\n"
      "ds_read_b128 v[216:219], v239 offset:3072\n"
      "v_rcp_f32 v184, v184\n"
      "v_rcp_f32 v185, v185\n"
      "v_rcp_f32 v186, v186\n"
      "v_rcp_f32 v187, v187\n"
      "v_mfma_f32_32x32x16_bf16 v[16:31], v[148:151], v[172:175], v[16:31]\n"
      "s_add_u32 m0, %[stg], 43008\n"
      "v_mul_f32 v196, v180, v196\n"
      "v_mul_f32 v197, v181, v197\n"
      "v_mul_f32 v198, v182, v198\n"
      "v_mul_f32 v199, v183, v199\n"
      "v_mul_f32 v200, v184, v200\n"
      "v_mul_f32 v201, v185, v201\n"
      "v_mul_f32 v202, v186, v202\n"
      "v_mul_f32 v203, v187, v203\n"
      "v_mfma_f32_32x32x16_bf16 v[48:63], v[148:151], v[188:191], v[48:63]\n"
      "s_sub_u32 s87, s87, 1\n"
      "global_load_lds_dwordx4 v222, s[88:89]\n"
      "global_load_lds_dwordx4 v223, s[88:89] offset:1024\n"
      "v_cvt_pk_bf16_f32 v180, v180, v181\n"
      "v_cvt_pk_bf16_f32 v181, v182, v183\n"
      "v_cvt_pk_bf16_f32 v182, v184, v185\n"
      "v_cvt_pk_bf16_f32 v183, v186, v187\n"
      "v_cvt_pk_bf16_f32 v196, v196, v197\n"
      "v_cvt_pk_bf16_f32 v197, v198, v199\n"
      "v_cvt_pk_bf16_f32 v198, v200, v201\n"
      "v_cvt_pk_bf16_f32 v199, v202, v203\n"
      "s_cbranch_scc1 2f\n"
      "s_setprio 1\n"
      "v_mfma_f32_32x32x16_bf16 v[16:31], v[152:155], v[180:183], v[16:31]\n"
      "ds_read_b64_tr_b16 v[148:149], v224 offset:2048\n"
      "v_exp_f32 v156, v156\n"
      "v_exp_f32 v157, v157\n"
      "v_exp_f32 v158, v158\n"
      "v_exp_f32 v159, v159\n"
      "v_mfma_f32_32x32x16_bf16 v[48:63], v[152:155], v[196:199], v[48:63]\n"
      "s_waitcnt lgkmcnt(1)\n"
      "ds_read_b64_tr_b16 v[150:151], v225 offset:2048\n"
      "v_exp_f32 v160, v160\n"
      "v_exp_f32 v161, v161\n"
      "v_exp_f32 v162, v162\n"
      "v_exp_f32 v163, v163\n"
      "v_mfma_f32_32x32x16_bf16 v[188:203], v[120:123], v[204:207], 0\n"
      "ds_read_b64_tr_b16 v[152:153], v226 offset:2048\n"
      "v_add_f32 v156, 1.0, v156\n"
      "v_add_f32 v157, 1.0, v157\n"
      "v_add_f32 v158, 1.0, v158\n"
      "v_add_f32 v159, 1.0, v159\n"
      "v_rcp_f32 v156, v156\n"
      "v_rcp_f32 v157, v157\n"
      "v_mfma_f32_32x32x16_bf16 v[188:203], v[124:127], v[208:211], v[188:203]\n"
      "ds_read_b64_tr_b16 v[154:155], v227 offset:2048\n"
      "v_rcp_f32 v158, v158\n"
      "v_rcp_f32 v159, v159\n"
      "v_add_f32 v160, 1.0, v160\n"
      "v_add_f32 v161, 1.0, v161\n"
      "v_add_f32 v162, 1.0, v162\n"
      "v_mfma_f32_32x32x16_bf16 v[188:203], v[128:131], v[212:215], v[188:203]\n"
      "ds_read_b32 v229, v232 offset:256\n"
      "v_add_f32 v163, 1.0, v163\n"
      "v_rcp_f32 v160, v160\n"
      "v_rcp_f32 v161, v161\n"
      "v_rcp_f32 v162, v162\n"
      "v_rcp_f32 v163, v163\n"
      "v_mfma_f32_32x32x16_bf16 v[188:203], v[132:135], v[216:219], v[188:203]\n"
      "ds_read_b128 v[224:227], v233 offset:256\n"
      "v_exp_f32 v164, v164\n"
      "v_add_u32 v232, s86, v232\n"
      "v_exp_f32 v165, v165\n"
      "v_exp_f32 v166, v166\n"
      "v_mfma_f32_32x32x16_bf16 v[172:187], v[136:139], v[100:103], 0\n"
      "ds_read_b128 v[220:223], v230 offset:512\n"
      "v_exp_f32 v167, v167\n"
      "v_add_u32 v233, s86, v233\n"
      "v_exp_f32 v168, v168\n"
      "v_exp_f32 v169, v169\n"
      "v_mfma_f32_32x32x16_bf16 v[172:187], v[140:143], v[112:115], v[172:187]\n"
      "ds_read_b32 v228, v231 offset:512\n"
      "v_exp_f32 v170, v170\n"
      "v_exp_f32 v171, v171\n"
      "v_add_u32 v230, s86, v230\n"
      "v_mul_f32 v188, v156, v188\n"
      "v_mul_f32 v189, v157, v189\n"
      "v_mul_f32 v190, v158, v190\n"
      "v_mfma_f32_32x32x16_bf16 v[172:187], v[144:147], v[116:119], v[172:187]\n"
      "s_waitcnt vmcnt(5) lgkmcnt(3)\n"
      "ds_read_b128 v[136:139], v238 offset:0\n"
      "v_mul_f32 v191, v159, v191\n"
      "v_mul_f32 v192, v160, v192\n"
      "v_mul_f32 v193, v161, v193\n"
      "v_and_or_b32 v229, v229, s84, v240\n"
      "v_mul_f32 v194, v162, v194\n"
      "v_mul_f32 v195, v163, v195\n"
      "v_cvt_pk_bf16_f32 v156, v156, v157\n"
      "v_cvt_pk_bf16_f32 v157, v158, v159\n"
      "v_mfma_f32_32x32x16_bf16 v[172:187], v[120:123], v[80:83], v[172:187]\n"
      "ds_read_b128 v[140:143], v229 offset:0\n"
      "ds_read_b128 v[144:147], v229 offset:32\n"
      "v_cvt_pk_bf16_f32 v158, v160, v161\n"
      "v_cvt_pk_bf16_f32 v159, v162, v163\n"
      "v_cvt_pk_bf16_f32 v188, v188, v189\n"
      "v_add_u32 v231, s86, v231\n"
      "v_cvt_pk_bf16_f32 v189, v190, v191\n"
      "v_cvt_pk_bf16_f32 v190, v192, v193\n"
      "v_cvt_pk_bf16_f32 v191, v194, v195\n"
      "v_add_f32 v164, 1.0, v164\n"
      "v_mfma_f32_32x32x16_bf16 v[172:187], v[124:127], v[84:87], v[172:187]\n"
      "ds_read_b128 v[204:207], v239 offset:4096\n"
      "v_add_f32 v165, 1.0, v165\n"
      "v_add_f32 v166, 1.0, v166\n"
      "v_add_f32 v167, 1.0, v167\n"
      "v_rcp_f32 v164, v164\n"
      "v_rcp_f32 v165, v165\n"
      "v_mfma_f32_32x32x16_bf16 v[172:187], v[128:131], v[88:91], v[172:187]\n"
      "ds_read_b128 v[208:211], v239 offset:5120\n"
      "v_rcp_f32 v166, v166\n"
      "v_rcp_f32 v167, v167\n"
      "v_add_f32 v168, 1.0, v168\n"
      "v_add_f32 v169, 1.0, v169\n"
      "v_add_f32 v170, 1.0, v170\n"
      "v_add_f32 v171, 1.0, v171\n"
      "v_mfma_f32_32x32x16_bf16 v[172:187], v[132:135], v[92:95], v[172:187]\n"
      "ds_read_b128 v[212:215], v239 offset:6144\n"
      "v_rcp_f32 v168, v168\n"
      "v_rcp_f32 v169, v169\n"
      "v_rcp_f32 v170, v170\n"
      "v_rcp_f32 v171, v171\n"
      "v_mfma_f32_32x32x16_bf16 v[0:15], v[148:151], v[156:159], v[0:15]\n"
      "s_cmp_gt_i32 s87, 2\n"
      "ds_read_b128 v[216:219], v239 offset:7168\n"
      "v_mul_f32 v196, v164, v196\n"
      "v_mul_f32 v197, v165, v197\n"
      "v_mul_f32 v198, v166, v198\n"
      "v_mul_f32 v199, v167, v199\n"
      "v_mul_f32 v200, v168, v200\n"
      "v_mul_f32 v201, v169, v201\n"
      "v_mul_f32 v202, v170, v202\n"
      "v_mul_f32 v203, v171, v203\n"
      "v_mfma_f32_32x32x16_bf16 v[32:47], v[148:151], v[188:191], v[32:47]\n"
      "s_cselect_b64 s[88:89], %[xs1], %[wu1]\n"
      "v_cvt_pk_bf16_f32 v164, v164, v165\n"
      "v_cvt_pk_bf16_f32 v165, v166, v167\n"
      "v_cvt_pk_bf16_f32 v166, v168, v169\n"
      "v_cvt_pk_bf16_f32 v167, v170, v171\n"
      "v_cvt_pk_bf16_f32 v196, v196, v197\n"
      "v_cvt_pk_bf16_f32 v197, v198, v199\n"
      "v_cvt_pk_bf16_f32 v198, v200, v201\n"
      "v_cvt_pk_bf16_f32 v199, v202, v203\n"
      "v_mfma_f32_32x32x16_bf16 v[0:15], v[152:155], v[164:167], v[0:15]\n"
      "v_exp_f32 v172, v172\n"
      "v_exp_f32 v173, v173\n"
      "v_exp_f32 v174, v174\n"
      "v_exp_f32 v175, v175\n"
      "v_mfma_f32_32x32x16_bf16 v[32:47], v[152:155], v[196:199], v[32:47]\n"
      "s_waitcnt lgkmcnt(3)\n"
      "v_exp_f32 v176, v176\n"
      "v_exp_f32 v177, v177\n"
      "v_lshrrev_b32 v228, 11, v228\n"
      "v_exp_f32 v178, v178\n"
      "v_exp_f32 v179, v179\n"
      "v_mfma_f32_32x32x16_bf16 v[188:203], v[120:123], v[204:207], 0\n"
      "s_waitcnt lgkmcnt(2)\n"
      "ds_read_b128 v[120:123], v234 offset:0\n"
      "v_add_f32 v172, 1.0, v172\n"
      "v_add_f32 v173, 1.0, v173\n"
      "v_lshl_add_u32 v228, v228, 5, v244\n"
      "v_add_f32 v174, 1.0, v174\n"
      "v_add_f32 v175, 1.0, v175\n"
      "v_rcp_f32 v172, v172\n"
      "v_mfma_f32_32x32x16_bf16 v[188:203], v[124:127], v[208:211], v[188:203]\n"
      "s_waitcnt lgkmcnt(2)\n"
      "ds_read_b128 v[124:127], v235 offset:0\n"
      "v_rcp_f32 v173, v173\n"
      "v_rcp_f32 v174, v174\n"
      "v_mad_u32_u16 v220, v220, s85, v242 op_sel:[1,0,0,0]\n"
      "v_rcp_f32 v175, v175\n"
      "v_add_f32 v176, 1.0, v176\n"
      "v_add_f32 v177, 1.0, v177\n"
      "v_mfma_f32_32x32x16_bf16 v[188:203], v[128:131], v[212:215], v[188:203]\n"
      "s_waitcnt lgkmcnt(2)\n"
      "ds_read_b128 v[128:131], v236 offset:0\n"
      "v_add_f32 v178, 1.0, v178\n"
      "v_add_f32 v179, 1.0, v179\n"
      "v_mad_u32_u16 v221, v221, s85, v243 op_sel:[1,0,0,0]\n"
      "v_rcp_f32 v176, v176\n"
      "v_rcp_f32 v177, v177\n"
      "v_mfma_f32_32x32x16_bf16 v[188:203], v[132:135], v[216:219], v[188:203]\n"
      "ds_read_b128 v[132:135], v237 offset:0\n"
      "v_rcp_f32 v178, v178\n"
      "v_rcp_f32 v179, v179\n"
      "v_mad_u32_u16 v222, v222, s85, v242 op_sel:[1,0,0,0]\n"
      "v_exp_f32 v180, v180\n"
      "v_exp_f32 v181, v181\n"
      "v_mfma_f32_32x32x16_bf16 v[156:171], v[140:143], v[104:107], 0\n"
      "v_exp_f32 v182, v182\n"
      "v_mad_u32_u16 v223, v223, s85, v243 op_sel:[1,0,0,0]\n"
      "v_exp_f32 v183, v183\n"
      "v_exp_f32 v184, v184\n"
      "v_mfma_f32_32x32x16_bf16 v[156:171], v[144:147], v[108:111], v[156:171]\n"
      "s_add_u32 m0, %[stg], 4096\n"
      "v_exp_f32 v185, v185\n"
      "v_exp_f32 v186, v186\n"
      "v_and_or_b32 v224, v224, s84, v241\n"
      "v_exp_f32 v187, v187\n"
      "v_mul_f32 v188, v172, v188\n"
      "v_mul_f32 v189, v173, v189\n"
      "v_mfma_f32_32x32x16_bf16 v[156:171], v[136:139], v[96:99], v[156:171]\n"
      "s_waitcnt lgkmcnt(3)\n"
      "v_mul_f32 v190, v174, v190\n"
      "v_mul_f32 v191, v175, v191\n"
      "v_mul_f32 v192, v176, v192\n"
      "v_and_or_b32 v225, v225, s84, v241\n"
      "v_mul_f32 v193, v177, v193\n"
      "v_mul_f32 v194, v178, v194\n"
      "v_mul_f32 v195, v179, v195\n"
      "v_cvt_pk_bf16_f32 v172, v172, v173\n"
      "v_mfma_f32_32x32x16_bf16 v[156:171], v[120:123], v[64:67], v[156:171]\n"
      "s_waitcnt lgkmcnt(0)\n"
      "global_load_lds_dwordx4 v228, %[ea] sc1\n"
      "ds_read_b128 v[204:207], v239 offset:0\n"
      "v_cvt_pk_bf16_f32 v173, v174, v175\n"
      "v_cvt_pk_bf16_f32 v174, v176, v177\n"
      "v_cvt_pk_bf16_f32 v175, v178, v179\n"
      "v_and_or_b32 v226, v226, s84, v241\n"
      "v_cvt_pk_bf16_f32 v188, v188, v189\n"
      "v_cvt_pk_bf16_f32 v189, v190, v191\n"
      "v_cvt_pk_bf16_f32 v190, v192, v193\n"
      "v_cvt_pk_bf16_f32 v191, v194, v195\n"
      "v_mfma_f32_32x32x16_bf16 v[156:171], v[124:127], v[68:71], v[156:171]\n"
      "s_mov_b32 m0, %[stg]\n"
      "ds_read_b128 v[208:211], v239 offset:1024\n"
      "v_add_f32 v180, 1.0, v180\n"
      "v_add_f32 v181, 1.0, v181\n"
      "v_add_f32 v182, 1.0, v182\n"
      "v_and_or_b32 v227, v227, s84, v241\n"
      "v_add_f32 v183, 1.0, v183\n"
      "v_rcp_f32 v180, v180\n"
      "v_rcp_f32 v181, v181\n"
      "s_setprio 0\n"
      "v_mfma_f32_32x32x16_bf16 v[156:171], v[128:131], v[72:75], v[156:171]\n"
      "global_load_lds_dwordx4 v220, s[88:89]\n"
      "ds_read_b128 v[212:215], v239 offset:2048\n"
      "v_rcp_f32 v182, v182\n"
      "v_rcp_f32 v183, v183\n"
      "v_add_f32 v184, 1.0, v184\n"
      "v_add_f32 v185, 1.0, v185\n"
      "v_add_f32 v186, 1.0, v186\n"
      "v_add_f32 v187, 1.0, v187\n"
      "v_mfma_f32_32x32x16_bf16 v[156:171], v[132:135], v[76:79], v[156:171]\n"
      "global_load_lds_dwordx4 v221, s[88:89] offset:1024\n"
      "ds_read_b128 v[216:219], v239 offset:3072\n"
      "v_rcp_f32 v184, v184\n"
      "v_rcp_f32 v185, v185\n"
      "v_rcp_f32 v186, v186\n"
      "v_rcp_f32 v187, v187\n"
      "v_mfma_f32_32x32x16_bf16 v[16:31], v[148:151], v[172:175], v[16:31]\n"
      "s_add_u32 m0, %[stg], 2048\n"
      "v_mul_f32 v196, v180, v196\n"
      "v_mul_f32 v197, v181, v197\n"
      "v_mul_f32 v198, v182, v198\n"
      "v_mul_f32 v199, v183, v199\n"
      "v_mul_f32 v200, v184, v200\n"
      "v_mul_f32 v201, v185, v201\n"
      "v_mul_f32 v202, v186, v202\n"
      "v_mul_f32 v203, v187, v203\n"
      "v_mfma_f32_32x32x16_bf16 v[48:63], v[148:151], v[188:191], v[48:63]\n"
      "s_sub_u32 s87, s87, 1\n"
      "global_load_lds_dwordx4 v222, s[88:89]\n"
      "global_load_lds_dwordx4 v223, s[88:89] offset:1024\n"
      "v_cvt_pk_bf16_f32 v180, v180, v181\n"
      "v_cvt_pk_bf16_f32 v181, v182, v183\n"
      "v_cvt_pk_bf16_f32 v182, v184, v185\n"
      "v_cvt_pk_bf16_f32 v183, v186, v187\n"
      "v_cvt_pk_bf16_f32 v196, v196, v197\n"
      "v_cvt_pk_bf16_f32 v197, v198, v199\n"
      "v_cvt_pk_bf16_f32 v198, v200, v201\n"
      "v_cvt_pk_bf16_f32 v199, v202, v203\n"
      "s_cbranch_scc0 4b\n"
      "2:\n"
      "s_setprio 0\n"
      "v_mfma_f32_32x32x16_bf16 v[16:31], v[152:155], v[180:183], v[16:31]\n"
      "s_nop 0\n"
      "v_mfma_f32_32x32x16_bf16 v[48:63], v[152:155], v[196:199], v[48:63]\n"
      "3:\n"
      "s_nop 7\n"
      "s_nop 4\n"
      : "=&{v[0:15]}"(yd[0]), "=&{v[16:31]}"(yd[1]), "=&{v[32:47]}"(yn[0]), "=&{v[48:63]}"(yn[1]), "=&{v[120:135]}"(axo), "+{v[230:233]}"(lp)
      : "{v[234:237]}"(aov), "{v[238:241]}"(mi1), "{v[242:245]}"(mi2), [l16] "v"(l16),
        [nt] "s"(ntile), [xs1] "s"(xs1), [wu1] "s"(wu1), [ea] "s"(ea), [stg] "s"(stg), [lb0] "s"(lb0), [wbs] "s"(wbase), [xbb] "s"(ldsbase + L_XB), [img] "s"(ldsbase + L_IMG), [wid] "s"(wid)
      : "v64", "v65", "v66", "v67", "v68", "v69", "v70", "v71", "v72", "v73", "v74", "v75", "v76", "v77", "v78", "v79", "v80", "v81", "v82", "v83", "v84", "v85", "v86", "v87", "v88", "v89", "v90", "v91", "v92", "v93", "v94", "v95", "v96", "v97", "v98", "v99", "v100", "v101", "v102", "v103", "v104", "v105", "v106", "v107", "v108", "v109", "v110", "v111", "v112", "v113", "v114", "v115", "v116", "v117", "v118", "v119", "v136", "v137", "v138", "v139", "v140", "v141", "v142", "v143", "v144", "v145", "v146", "v147", "v148", "v149", "v150", "v151", "v152", "v153", "v154", "v155", "v156", "v157", "v158", "v159", "v160", "v161", "v162", "v163", "v164", "v165", "v166", "v167", "v168", "v169", "v170", "v171", "v172", "v173", "v174", "v175", "v176", "v177", "v178", "v179", "v180", "v181", "v182", "v183", "v184", "v185", "v186", "v187", "v188", "v189", "v190", "v191", "v192", "v193", "v194", "v195", "v196", "v197", "v198", "v199", "v200", "v201", "v202", "v203", "v204", "v205", "v206", "v207", "v208", "v209", "v210", "v211", "v212", "v213", "v214", "v215", "v216", "v217", "v218", "v219", "v220", "v221", "v222", "v223", "v224", "v225", "v226", "v227", "v228", "v229", "s84", "s85", "s86", "s87", "s88", "s89", "scc", "memory");
  {
#define DSR128(d, a, o) asm volatile("ds_read_b128 %0, %1 offset:%2" : "=v"(d) : "v"(a), "n"(o) : "memory")
    const int ln = (int)__builtin_amdgcn_mbcnt_hi(~0u, __builtin_amdgcn_mbcnt_lo(~0u, 0u));
    const int rr32 = ln & 31, hh = ln >> 5;
    const int wcnt = WSTART(8 * b + win + 1) - wabs;
    bf16x8 xe[4];
#pragma unroll
    for (int s = 0; s < 4; ++s) xe[s] = *(const bf16x8*)(smem + L_XB + 128 * (wbase + rr32) + 16 * ((2 * s + hh) ^ (((wbase + rr32) >> 1) & 7)));
    u32x4 ax[4];
    asm volatile("s_waitcnt vmcnt(5)" ::: "memory");
    const u32 ub = (u32)(((ntile + 1) & 1) * STG_BUF);
#pragma unroll
    for (int s = 0; s < 4; ++s) DSR128(ax[s], aov[s] + ub, 0);
    u32 xv[16];
#pragma unroll
    for (int j = 0; j < 4; ++j) {
      const int row = wbase + 4 * hh + j;
      const int a = L_XB + 128 * row + 16 * ((rr32 >> 2) ^ ((row >> 1) & 7)) + 4 * (rr32 & 3);
      xv[j] = *(const u32*)(smem + a); xv[4 + j] = *(const u32*)(smem + (a ^ 64) + 1024);
      xv[8 + j] = *(const u32*)(smem + a + 2048); xv[12 + j] = *(const u32*)(smem + (a ^ 64) + 3072);
    }
    f32x16 zu[2] = {};
#pragma unroll
    for (int s = 0; s < 4; ++s) zu[0] = MFMA32(xe[s], BC8(((u32x4){axo[4 * s], axo[4 * s + 1], axo[4 * s + 2], axo[4 * s + 3]})), zu[0]);
#pragma unroll
    for (int i = 0; i < 16; ++i) yd[0][i] = __builtin_amdgcn_rcpf(yd[0][i] + 1e-6f);
    asm volatile("s_waitcnt lgkmcnt(0)" : "+v"(ax[0]), "+v"(ax[1]), "+v"(ax[2]), "+v"(ax[3]), "+v"(yd[0]) :: "memory");
#pragma unroll
    for (int s = 0; s < 4; ++s) zu[1] = MFMA32(xe[s], BC8(ax[s]), zu[1]);
#pragma unroll
    for (int i = 0; i < 16; ++i) yd[1][i] = __builtin_amdgcn_rcpf(yd[1][i] + 1e-6f);
    asm volatile("s_waitcnt vmcnt(0)" : "+v"(yd[1]) :: "memory");
    const __amdgpu_buffer_rsrc_t ro = __builtin_amdgcn_make_buffer_rsrc((void*)(out + (size_t)wabs * 32), 0, wcnt * 128, 0x00020000);
#pragma unroll
    for (int i = 0; i < 16; ++i) {
      const float z0 = zu[0][i] + yn[0][i] * yd[0][i];
      const float z1 = zu[1][i] + yn[1][i] * yd[1][i];
      const float o0 = U2F(xv[i] << 16) + __builtin_fmaxf(z0, 0.f);
      const float o1 = U2F(xv[i] & 0xFFFF0000u) + __builtin_fmaxf(z1, 0.f);
      __builtin_amdgcn_raw_buffer_store_b32(cvtpk(o0, o1), ro, 512 * hh + 4 * rr32 + 128 * ((i & 3) + 8 * (i >> 2)), 0, OUTAUX);
    }
  }
}

__global__ void fb_proj_kernel(const u16* x, const u16* wa, const u16* wb, const u16* wv, const u16* wu, float* proj,
                               float* acc, int N) {
  int n = blockIdx.x;
  int t = threadIdx.x;
  int m = t >> 6, f = t & 63;
  const u16* w = m == 0 ? wa : m == 1 ? wb : m == 2 ? wv : wu;
  float s = 0.f;
  for (int k = 0; k < 64; ++k) s += bf2g(x[(size_t)n * 64 + k]) * bf2g(w[k * 64 + f]);
  proj[(size_t)n * 256 + t] = s;
  if (t < 128) acc[(size_t)n * 128 + t] = 0.f;
}
__global__ void fb_edge_kernel(const u16* ea, const int* src, const int* dst, const u16* wc, const float* proj, float* acc,
                               int E) {
  int e = blockIdx.x * 4 + (threadIdx.x >> 6);
  int f = threadIdx.x & 63;
  if (e >= E) return;
  int s = src[e], d = dst[e];
  float g = proj[(size_t)d * 256 + f] + proj[(size_t)s * 256 + 64 + f];
  for (int k = 0; k < 16; ++k) g += bf2g(ea[(size_t)e * 16 + k]) * bf2g(wc[k * 64 + f]);
  float eta = 1.f / (1.f + __expf(-g));
  atomicAdd(&acc[(size_t)d * 128 + f], eta);
  atomicAdd(&acc[(size_t)d * 128 + 64 + f], eta * proj[(size_t)s * 256 + 128 + f]);
}
__global__ void fb_fin_kernel(const u16* x, const float* proj, const float* acc, __hip_bfloat16* out, int N) {
  size_t i = (size_t)blockIdx.x * 256 + threadIdx.x;
  if (i >= (size_t)N * 64) return;
  size_t n = i >> 6, f = i & 63;
  float agg = acc[n * 128 + 64 + f] / (acc[n * 128 + f] + 1e-6f);
  float z = proj[n * 256 + 192 + f] + agg;
  out[i] = __float2bfloat16(bf2g(x[i]) + (z > 0.f ? z : 0.f));
}

extern "C" void kernel_launch(void* const* d_in, const int* in_sizes, int n_in, void* d_out, int out_size, void* d_ws,
                              size_t ws_size, hipStream_t stream) {
  const u16* x = (const u16*)d_in[0];
  const u16* ea = (const u16*)d_in[1];
  const int* src = (const int*)d_in[2];
  const int* dst = (const int*)d_in[3];
  const u16* wa = (const u16*)d_in[4];
  const u16* wb = (const u16*)d_in[5];
  const u16* wc = (const u16*)d_in[6];
  const u16* wu = (const u16*)d_in[7];
  const u16* wv = (const u16*)d_in[8];
  const int N = in_sizes[0] / 64, E = in_sizes[2];
  const int WN = (N + 8 * NBK - 1) / (8 * NBK);
  int CH = (E + G1 - 1) / G1; CH = (CH + 3) & ~3;
  int dev = 0, ncu = 0;
  (void)hipGetDevice(&dev);
  (void)hipDeviceGetAttribute(&ncu, hipDeviceAttributeMultiprocessorCount, dev);
  const bool fast = WN >= 2 && WN <= WNMAX && CH <= CHMAX && CH >= 4 && E <= (1 << 21) && ncu >= NBK &&
                    (((uintptr_t)src | (uintptr_t)dst | (uintptr_t)x | (uintptr_t)ea) & 15) == 0 &&
                    (((uintptr_t)wa | (uintptr_t)wb | (uintptr_t)wc | (uintptr_t)wu | (uintptr_t)wv) & 3) == 0;
  if (!fast) {
    float* proj = (float*)d_ws;
    float* acc = proj + (size_t)N * 256;
    if (N > 0) fb_proj_kernel<<<N, 256, 0, stream>>>(x, wa, wb, wv, wu, proj, acc, N);
    if (E > 0) fb_edge_kernel<<<(E + 3) / 4, 256, 0, stream>>>(ea, src, dst, wc, proj, acc, E);
    if (N > 0) fb_fin_kernel<<<(N * 64 + 255) / 256, 256, 0, stream>>>(x, proj, acc, (__hip_bfloat16*)d_out, N);
    return;
  }
  const u32 magw = (u32)((((u64)1 << 43) + (u64)N - 1) / (u64)N - 1);
  const int H = N / 2;
  (void)hipFuncSetAttribute((const void*)main_kernel, hipFuncAttributeMaxDynamicSharedMemorySize, L_TOTAL);
  main_kernel<<<NBK, 512, L_TOTAL, stream>>>((u32x4*)d_ws, x, ea, (u32*)d_out, N, CH, H, E, src, dst, wa, wb, wc, wu, wv, magw, 0
  );
}
```
